```python
import torch

def _softmax(x, axis=-1):
    return torch.softmax(x, dim=axis)
def _gelu(x):
    return torch.nn.functional.gelu(x, approximate="tanh")
def _silu(x):
    return torch.nn.functional.silu(x)
def _sigmoid(x):
    return torch.sigmoid(x)
def _layernorm(x, axis=-1, eps=1e-5):
    m = x.mean(dim=axis, keepdim=True)
    v = x.var(dim=axis, keepdim=True, unbiased=False)
    return (x - m) / torch.sqrt(v + eps)
def _scatter_add(tgt, idx, src, axis=0):
    return tgt.clone().index_add_(axis, idx, src)
def _segment_sum(src, seg, n):
    out = torch.zeros((n,) + src.shape[1:], dtype=src.dtype)
    return out.index_add_(0, seg, src)
def _pad(x, pw):
    flat = tuple(v for pair in reversed(pw) for v in pair)
    return torch.nn.functional.pad(x, flat)

kernel_name = 'sequential_preLN__linear_attn_block__geglu_ffn__prompt'

def setup_inputs():

    _w = (lambda a: (lambda t: t.to(torch.bfloat16) if t.is_floating_point() else t)(torch.as_tensor(a)))
    def _A_setup():
        B, S, D = 1, 2048, 1024
        return {
            "x": _w((lambda *s: torch.randn(*s, dtype=torch.bfloat16) * 0.05)(B, S, D)),
            "wqkv": _w((lambda *s: torch.randn(*s, dtype=torch.bfloat16) * 0.05)(D, 3 * D)),
            "wo": _w((lambda *s: torch.randn(*s, dtype=torch.bfloat16) * 0.05)(D, D)),
        }
    _A_in = _A_setup()

    def _B_setup():
        B, S, D, F = 1, 2048, 1024, 3584
        return {
            "x": _w((lambda *s: torch.randn(*s, dtype=torch.bfloat16) * 0.05)(B, S, D)),
            "w_gate": _w((lambda *s: torch.randn(*s, dtype=torch.bfloat16) * 0.05)(D, F)),
            "w_up": _w((lambda *s: torch.randn(*s, dtype=torch.bfloat16) * 0.05)(D, F)),
            "w_down": _w((lambda *s: torch.randn(*s, dtype=torch.bfloat16) * 0.05)(F, D)),
        }
    _B_in = _B_setup()

    return {
        "A_x": _A_in["x"],
        "A_wqkv": _A_in["wqkv"],
        "A_wo": _A_in["wo"],
        "B_x": _B_in["x"],
        "B_w_gate": _B_in["w_gate"],
        "B_w_up": _B_in["w_up"],
        "B_w_down": _B_in["w_down"],
    }

def reference(A_x, A_wqkv, A_wo, B_x, B_w_gate, B_w_up, B_w_down):
    def _A_ref(x, wqkv, wo):
        B, S, D = x.shape
        H = 32; Dh = D // H
        qkv = (lambda x, *d: x.permute(*d))((x @ wqkv).reshape(B, S, 3, H, Dh), 2, 0, 3, 1, 4)
        q, k, v = qkv[0], qkv[1], qkv[2]
        phi = lambda t: torch.where(t > 0, t + 1, torch.exp(t)) + 1e-6
        pq, pk = phi(q), phi(k)
        kv = torch.einsum("bhsd,bhse->bhde", pk, v)
        z = torch.einsum("bhsd->bhd", pk)
        num = torch.einsum("bhqd,bhde->bhqe", pq, kv)
        den = torch.einsum("bhqd,bhd->bhq", pq, z)[..., None] + 1e-6
        o = num / den
        return (lambda x, *d: x.permute(*d))(o, 0, 2, 1, 3).reshape(B, S, D) @ wo

    def _B_ref(x, w_gate, w_up, w_down):
        return (_gelu(x @ w_gate) * (x @ w_up)) @ w_down

    x = A_x
    h = x + _A_ref(_layernorm(x, axis=-1), A_wqkv, A_wo)
    h2 = h + _B_ref(_layernorm(h, axis=-1), B_w_gate, B_w_up, B_w_down)
    return h2
```

```cpp
#include <hip/hip_runtime.h>
#include <stdint.h>

typedef __attribute__((ext_vector_type(4))) short s16x4;
typedef __attribute__((ext_vector_type(8))) short s16x8;
typedef __attribute__((ext_vector_type(8))) __bf16 bf16x8;
typedef __attribute__((ext_vector_type(2))) __bf16 bf16x2;
typedef __attribute__((ext_vector_type(4))) float f32x4;
typedef __attribute__((ext_vector_type(2))) float f32x2;
typedef __attribute__((ext_vector_type(4))) unsigned u32x4;
typedef __attribute__((ext_vector_type(2))) unsigned u32x2;
#define LDS3(T, a) ((__attribute__((address_space(3))) T*)(size_t)(a))
#define DEVI __device__ __forceinline__

template <int N> DEVI void wait_vm() { asm volatile("s_waitcnt vmcnt(%0)" ::"n"(N) : "memory"); }
DEVI void wait_lgkm0() { asm volatile("s_waitcnt lgkmcnt(0)" ::: "memory"); }
DEVI void bar() { __builtin_amdgcn_s_barrier(); }
DEVI void glds16(unsigned voff, const char* sbase, unsigned lds) {
  asm volatile("s_mov_b32 m0, %2\n\ts_nop 0\n\tglobal_load_lds_dwordx4 %0, %1" ::"v"(voff), "s"(sbase), "s"(lds) : "memory");
}
DEVI bf16x8 lds_b128(unsigned a) { return *LDS3(bf16x8, a); }
DEVI s16x4 lds_tr(unsigned a) { return __builtin_amdgcn_ds_read_tr16_b64_v4i16(LDS3(s16x4, a)); }
DEVI unsigned pack_bf16(float lo, float hi) {
  bf16x2 v; v[0] = (__bf16)lo; v[1] = (__bf16)hi; return __builtin_bit_cast(unsigned, v);
}
DEVI float bf_lo(unsigned u) { return __uint_as_float(u << 16); }
DEVI float bf_hi(unsigned u) { return __uint_as_float(u & 0xffff0000u); }
DEVI float dot2(unsigned a, unsigned b, float c) {
  return __builtin_amdgcn_fdot2_f32_bf16(__builtin_bit_cast(bf16x2, a), __builtin_bit_cast(bf16x2, b), c, false);
}
DEVI void swap16(float& a, float& b) {
  auto r = __builtin_amdgcn_permlane16_swap(__float_as_uint(a), __float_as_uint(b), false, false);
  a = __uint_as_float(r[0]); b = __uint_as_float(r[1]);
}
DEVI void swap16u(unsigned& a, unsigned& b) {
  auto r = __builtin_amdgcn_permlane16_swap(a, b, false, false);
  a = r[0]; b = r[1];
}
DEVI void swap32(float& a, float& b) {
  auto r = __builtin_amdgcn_permlane32_swap(__float_as_uint(a), __float_as_uint(b), false, false);
  a = __uint_as_float(r[0]); b = __uint_as_float(r[1]);
}
DEVI float sum_g(float x) {
  float a = x, b = x; swap16(a, b); x = a + b;
  a = x; b = x; swap32(a, b); return a + b;
}
DEVI f32x4 mfma16(bf16x8 a, bf16x8 b, f32x4 c) { return __builtin_amdgcn_mfma_f32_16x16x32_bf16(a, b, c, 0, 0, 0); }

template <int BM_, int BN_, int BK_, int S_, int WM_, int WN_, int WK_, int NK_, int AMODE_, int BMODE_>
struct Cfg {
  static constexpr int BM = BM_, BN = BN_, BK = BK_, S = S_, WM = WM_, WN = WN_, WK = WK_, NK = NK_, AMODE = AMODE_, BMODE = BMODE_;
  static constexpr int NW = WM * WN * WK;
  static constexpr int MF = BM / WM / 16, NF = BN / WN / 16, KF = BK / 32, KFW = KF / WK;
  static constexpr int A_BYTES = BM * BK * 2;
  static constexpr int B_BYTES = BN * BK * 2 + (BMODE == 1 ? 256 : 0);
  static constexpr int STAGE = A_BYTES + B_BYTES;
  static constexpr int PA = A_BYTES / 1024, PB = BN * BK * 2 / 1024, P = PA + PB;
  static constexpr int NP = (P + NW - 1) / NW;
  static constexpr int FULLW = P - (NP - 1) * NW;
  static constexpr int RS = (BMODE == 1 ? BN : BN * 2);
  static constexpr int LDS = S * STAGE;
  static constexpr bool SLOT = (MF == 2);
};

template <class C> struct Frags { bf16x8 a[C::KFW][C::MF]; bf16x8 b[C::KFW][C::NF]; };
template <class C> struct Loader { const char* sbase[C::NP]; unsigned voff[C::NP]; unsigned ldso[C::NP]; unsigned sstr[C::NP]; };

template <class C, class ColMap>
DEVI void loader_init(Loader<C>& L, int wave, int lane, const void* A, int m0, int lda, const void* B0, const void* B1, int ldb, int k0,
                      ColMap colmap) {
#pragma unroll
  for (int i = 0; i < C::NP; ++i) {
    int p = wave + i * C::NW;
    if (p >= C::P) p = C::P - 1;
    if (p < C::PA) {
      if (C::AMODE == 0) {
        int mb = p / C::KF, kb = p % C::KF;
        L.sbase[i] = (const char*)A + ((size_t)(m0 / 16 + mb) * (lda / 8) + (k0 / 8 + kb * 4)) * 256;
        L.voff[i] = lane * 16;
        L.sstr[i] = C::BK / 8 * 256;
      } else {
        int row = 8 * p + (lane >> 3), pos = lane & 7, lc = pos ^ ((row >> 1) & 7);
        L.sbase[i] = (const char*)A + ((size_t)(m0 + 8 * p) * lda + k0) * 2;
        L.voff[i] = ((lane >> 3) * lda + lc * 8) * 2;
        L.sstr[i] = C::BK * 2;
      }
      L.ldso[i] = p * 1024;
    } else {
      int pb = p - C::PA;
      if (C::BMODE == 0) {
        constexpr int CH = C::BN / 8;
        int ci = pb * 64 + lane, k = ci / CH, c = ci % CH, u = c >> 1;
        int sg = (C::RS % 256 == 0) ? ((k & 3) | (((k >> 3) & 1) << 2)) : (C::RS % 256 == 128) ? (((k >> 1) & 1) | (((k >> 3) & 1) << 1)) : ((k >> 3) & 1);
        int lc = ((u ^ sg) << 1) | (c & 1);
        L.sbase[i] = (const char*)B0 + (size_t)k0 * ldb * 2;
        L.voff[i] = (k * ldb + colmap(lc)) * 2;
        L.ldso[i] = C::A_BYTES + pb * 1024;
      } else {
        constexpr int CH = C::BN / 16, HP = C::PB / 2;
        int half = pb / HP, pp = pb % HP;
        int ci = pp * 64 + lane, k = ci / CH, c = ci % CH, lc = c ^ ((k >> 3) & 1);
        L.sbase[i] = (const char*)(half ? B1 : B0) + (size_t)k0 * ldb * 2;
        L.voff[i] = (k * ldb + colmap(lc)) * 2;
        L.ldso[i] = C::A_BYTES + half * (HP * 1024 + 128) + pp * 1024;
      }
      L.sstr[i] = C::BK * ldb * 2;
    }
    {
      unsigned long long u = (unsigned long long)L.sbase[i];
      unsigned lo = __builtin_amdgcn_readfirstlane((unsigned)u), hi = __builtin_amdgcn_readfirstlane((unsigned)(u >> 32));
      L.sbase[i] = (const char*)(((unsigned long long)hi << 32) | lo);
      L.ldso[i] = __builtin_amdgcn_readfirstlane(L.ldso[i]);
      L.sstr[i] = __builtin_amdgcn_readfirstlane(L.sstr[i]);
    }
  }
}

template <class C> DEVI void issue_piece(Loader<C>& L, int i, int wave, unsigned bufbase) {
  if (i < C::NP - 1 || C::FULLW == C::NW || wave < C::FULLW) glds16(L.voff[i], L.sbase[i], bufbase + L.ldso[i]);
  L.sbase[i] += L.sstr[i];
}
template <class C, int NST> DEVI void wait_stages(int wave) {
  if (C::FULLW == C::NW || wave < C::FULLW) wait_vm<NST * C::NP>(); else wait_vm<NST*(C::NP - 1)>();
}

template <class C> struct RdAddr { unsigned a[C::KFW][2]; unsigned b[C::NF]; unsigned kbo[2]; };
template <class C> DEVI void rd_init(RdAddr<C>& R, unsigned lds0, int wm, int wn, int wk, int lane, int mx, int kx, int jx) {
  int g = lane >> 4, r15 = lane & 15, q = (lane & 15) >> 2, p = lane & 3;
#pragma unroll
  for (int kb = 0; kb < C::KFW; ++kb)
#pragma unroll
    for (int m = 0; m < 2; ++m) {
      int ka = wk * C::KFW + (kb ^ kx), ma = wm * C::MF + (m ^ mx);
      if (C::AMODE == 0) R.a[kb][m] = lds0 + (ma * C::KF + ka) * 1024 + lane * 16;
      else R.a[kb][m] = lds0 + (ma * 16 + r15) * 128 + (((ka * 4 + g) ^ ((r15 >> 1) & 7)) * 16);
    }
  R.kbo[0] = kx * 32 * C::RS; R.kbo[1] = (1 ^ kx) * 32 * C::RS;
  int krow = wk * C::KFW * 32 + 8 * g + q;
#pragma unroll
  for (int j = 0; j < C::NF; ++j) {
    int U = wn * C::NF + (j ^ jx);
    if (C::BMODE == 0) {
      int sg = (C::RS % 256 == 0) ? (q | ((g & 1) << 2)) : (C::RS % 256 == 128) ? ((q >> 1) | ((g & 1) << 1)) : (g & 1);
      R.b[j] = lds0 + C::A_BYTES + krow * C::RS + ((U ^ sg) * 32) + p * 8;
    } else {
      R.b[j] = lds0 + C::A_BYTES + (p >> 1) * (C::PB / 2 * 1024 + 128) + krow * C::RS + ((U ^ (g & 1)) * 16) + (p & 1) * 8;
    }
  }
}
template <class C> DEVI void read_a(Frags<C>& f, const RdAddr<C>& R, unsigned so, int kb, int mf) {
  if (C::SLOT) f.a[kb][mf] = lds_b128(R.a[kb][mf & 1] + so);
  else f.a[kb][mf] = lds_b128(R.a[0][0] + so + (mf * C::KF + kb) * 1024);
}
template <class C> DEVI void read_b(Frags<C>& f, const RdAddr<C>& R, unsigned so, int kb, int j) {
  unsigned a = (C::AMODE == 1) ? R.b[j] + (so + R.kbo[kb & 1]) : R.b[j] + so + (kb * 32) * C::RS;
  s16x4 lo = lds_tr(a);
  s16x4 hi = lds_tr(a + 4 * C::RS);
  s16x8 v = __builtin_shufflevector(lo, hi, 0, 1, 2, 3, 4, 5, 6, 7);
  f.b[kb][j] = __builtin_bit_cast(bf16x8, v);
}
template <class C> DEVI void read_all(Frags<C>& f, const RdAddr<C>& R, unsigned so) {
#pragma unroll
  for (int kb = 0; kb < C::KFW; ++kb) {
#pragma unroll
    for (int m = 0; m < C::MF; ++m) read_a<C>(f, R, so, kb, m);
#pragma unroll
    for (int j = 0; j < C::NF; ++j) read_b<C>(f, R, so, kb, j);
  }
}

template <class C, unsigned SWAPMASK, bool ISSUE, bool READ, int NOUT, class Hook>
DEVI void iter(f32x4 (&acc)[C::MF][C::NF], const Frags<C>& cur, Frags<C>& nxt, Loader<C>& L, const RdAddr<C>& R, int wave, unsigned lds0,
               unsigned& bcur, Hook& hook) {
  wait_lgkm0();
  if (READ) { wait_stages<C, NOUT>(wave); }
  bar();
  unsigned bn = bcur + C::STAGE; if (bn == C::LDS) bn = 0;
  constexpr int NM = C::KFW * C::MF * C::NF;
  constexpr int NR = C::KFW * (C::MF + C::NF);
  constexpr int SEG = C::NP + 1;
#pragma unroll
  for (int s = 0; s < SEG; ++s) {
    if (READ) {
#pragma unroll
      for (int r = s * NR / SEG; r < (s + 1) * NR / SEG; ++r) {
        int kb = r / (C::MF + C::NF), x = r % (C::MF + C::NF);
        if (x < C::MF) read_a<C>(nxt, R, bn, kb, x); else read_b<C>(nxt, R, bn, kb, x - C::MF);
      }
    }
#pragma unroll
    for (int i = s * NM / SEG; i < (s + 1) * NM / SEG; ++i) {
      int kb = i / (C::MF * C::NF), x = i % (C::MF * C::NF), j = x / C::MF, m = x % C::MF;
      if ((SWAPMASK >> j) & 1) acc[m][j] = mfma16(cur.b[kb][j], cur.a[kb][m], acc[m][j]);
      else acc[m][j] = mfma16(cur.a[kb][m], cur.b[kb][j], acc[m][j]);
    }
    hook.seg(cur, s, SEG);
#pragma unroll
    for (int x = 0; x < 8; ++x) {
      __builtin_amdgcn_sched_group_barrier(0x8, 1, 0);
      __builtin_amdgcn_sched_group_barrier(0x100, 1, 0);
      if (Hook::VALU) __builtin_amdgcn_sched_group_barrier(0x2, Hook::VALU, 0);
    }
    __builtin_amdgcn_sched_barrier(0);
    if (ISSUE && s < C::NP) { issue_piece<C>(L, s, wave, lds0 + bcur); __builtin_amdgcn_sched_barrier(0); }
  }
  bcur = bn;
}

template <class C> DEVI void prologue(Loader<C>& L, int wave, unsigned lds0) {
#pragma unroll
  for (int s = 0; s < C::S; ++s)
#pragma unroll
    for (int i = 0; i < C::NP; ++i) issue_piece<C>(L, i, wave, lds0 + s * C::STAGE);
}

template <class C, unsigned SWAPMASK, class Hook>
DEVI void mainloop(f32x4 (&acc)[C::MF][C::NF], Loader<C>& L, const RdAddr<C>& R, int wave, unsigned lds0, Hook& hook) {
  constexpr int S = C::S, NK = C::NK;
  Frags<C> f0, f1;
  wait_stages<C, S - 1>(wave);
  bar();
  read_all<C>(f0, R, 0);
  unsigned bcur = 0;
  constexpr int NMAIN = NK - S;
  constexpr int NPAIR = NMAIN / 2;
  for (int it = 0; it < NPAIR; ++it) {
    iter<C, SWAPMASK, true, true, S - 2>(acc, f0, f1, L, R, wave, lds0, bcur, hook);
    iter<C, SWAPMASK, true, true, S - 2>(acc, f1, f0, L, R, wave, lds0, bcur, hook);
  }
  constexpr int T0 = 2 * NPAIR;
#define TAILIT(T, CUR, NXT)                                                        \
  if constexpr (T0 + (T) < NK) {                                                   \
    constexpr int kt = T0 + (T);                                                   \
    constexpr bool iss = kt + S < NK, rd = kt + 1 < NK;                            \
    constexpr int last_issued = (kt + S - 1 < NK - 1) ? kt + S - 1 : NK - 1;       \
    constexpr int nout = rd ? last_issued - (kt + 1) : 0;                          \
    iter<C, SWAPMASK, iss, rd, nout>(acc, CUR, NXT, L, R, wave, lds0, bcur, hook); \
  }
  TAILIT(0, f0, f1) TAILIT(1, f1, f0) TAILIT(2, f0, f1) TAILIT(3, f1, f0) TAILIT(4, f0, f1) TAILIT(5, f1, f0) TAILIT(6, f0, f1) TAILIT(7, f1, f0)
#undef TAILIT
}
struct NoHook { static constexpr int VALU = 0; template <class F> DEVI void seg(const F&, int, int) const {} };

constexpr int SEQ = 2048, DM = 1024, FF = 3584;
constexpr size_t MB = 1u << 20;
constexpr size_t WS_QF = 0;
constexpr size_t WS_OT = 4 * MB;
constexpr size_t WS_HT = 8 * MB;
constexpr size_t WS_KVP = 12 * MB;
constexpr size_t WS_ZP = 14 * MB;
constexpr size_t WS_ST = 14 * MB + 256 * 1024;
constexpr size_t WS_CS = 14 * MB + 768 * 1024;
constexpr size_t WS_ACT = 16 * MB;
constexpr unsigned ONES2 = 0x3f803f80u;

DEVI float phi(float t) { return (t > 0.f ? t + 1.f : __expf(t)) + 1e-6f; }

using CA = Cfg<128, 192, 64, 3, 4, 2, 1, 16, 1, 0>;
struct HookA {
  static constexpr int VALU = 2;
  float rs, rq, c[3];
  DEVI void sum4(const bf16x8& f, float& a) { u32x4 u = __builtin_bit_cast(u32x4, f);
#pragma unroll
    for (int i = 0; i < 4; ++i) a = dot2(u[i], ONES2, a); }
  DEVI void sq4(const bf16x8& f, float& a) { u32x4 u = __builtin_bit_cast(u32x4, f);
#pragma unroll
    for (int i = 0; i < 4; ++i) a = dot2(u[i], u[i], a); }
  template <class F> DEVI void seg(const F& f, int s, int SEG) {
#pragma unroll
    for (int u = s * 7 / SEG; u < (s + 1) * 7 / SEG; ++u) {
      if (u == 0) sum4(f.a[0][0], rs);
      if (u == 1) sq4(f.a[0][0], rq);
      if (u == 2) sum4(f.b[0][0], c[0]);
      if (u == 3) sum4(f.b[0][2], c[1]);
      if (u == 4) sum4(f.b[0][4], c[2]);
      if (u == 5) sum4(f.a[1][0], rs);
      if (u == 6) sq4(f.a[1][0], rq);
    }
  }
};

__global__ __launch_bounds__(512, 2) void kA(const void* __restrict__ x, const void* __restrict__ wqkv, char* __restrict__ ws) {
  using C = CA;
  __shared__ __attribute__((aligned(256))) char sm[C::LDS];
  unsigned lds0 = (unsigned)(size_t)sm;
  int tid = threadIdx.x, lane = tid & 63, wave = __builtin_amdgcn_readfirstlane(tid >> 6);
  int wn = wave & 1, wm = wave >> 1;
  int b = blockIdx.x, xcd = b & 7, idx = b >> 3;
  int tm = (xcd >> 1) * 4 + (idx & 3), tn = (xcd & 1) * 8 + (idx >> 2);
  Loader<C> L;
  loader_init<C>(L, wave, lane, x, tm * 128, DM, wqkv, wqkv, 3 * DM, 0, [&](int lc) {
    int c = lc * 8, w = c / 96, r = c % 96;
    return (r >> 5) * DM + (2 * tn + w) * 32 + (r & 31);
  });
  prologue<C>(L, wave, lds0);
  int mx = wn, kx = wm & 1, jx = wm >> 1;
  RdAddr<C> R; rd_init<C>(R, lds0, wm, wn, 0, lane, mx, kx, jx);
  f32x4 acc[2][6];
#pragma unroll
  for (int m = 0; m < 2; ++m)
#pragma unroll
    for (int j = 0; j < 6; ++j) acc[m][j] = f32x4{0, 0, 0, 0};
  HookA hk; hk.rs = 0; hk.rq = 0; hk.c[0] = hk.c[1] = hk.c[2] = 0;
  mainloop<C, 0x3u>(acc, L, R, wave, lds0, hk);

  int g = lane >> 4, r15 = lane & 15;
  constexpr unsigned O_ROW = 0, O_CS = 1024, O_Z = 4096, O_RED = 8192;
  {
    float s = sum_g(hk.rs), q = sum_g(hk.rq);
    float mean = s * (1.f / DM), var = fmaxf(q * (1.f / DM) - mean * mean, 0.f);
    float rstd = __builtin_amdgcn_rsqf(var + 1e-5f);
    float c0 = sum_g(hk.c[0]), c1 = sum_g(hk.c[1]), c2 = sum_g(hk.c[2]);
    if (g == 0) *LDS3(f32x2, lds0 + O_ROW + (wm * 32 + wn * 16 + r15) * 8) = f32x2{mean, rstd};
    float cc = g == 1 ? c0 : g == 2 ? c1 : c2;
    if (g) *LDS3(float, lds0 + O_CS + (kx * 192 + wn * 96 + (((g - 1) * 2) ^ jx) * 16 + r15) * 4) = cc;
  }
  wait_lgkm0(); bar();
  int head = 2 * tn + wn;
#pragma unroll
  for (int m = 0; m < 2; ++m) {
    int ma = m ^ mx;
    f32x2 st = *LDS3(f32x2, lds0 + O_ROW + (wm * 32 + ma * 16 + r15) * 8);
    unsigned pk[2][2];
#pragma unroll
    for (int j = 0; j < 2; ++j) {
      unsigned ca = lds0 + O_CS + (wn * 96 + (j ^ jx) * 16 + 4 * g) * 4;
      f32x4 cs = *LDS3(f32x4, ca) + *LDS3(f32x4, ca + 768);
      float v[4];
#pragma unroll
      for (int r = 0; r < 4; ++r) v[r] = phi((acc[m][j][r] - st[0] * cs[r]) * st[1]);
      pk[j][0] = pack_bf16(v[0], v[1]); pk[j][1] = pack_bf16(v[2], v[3]);
    }
    u32x4 o = jx ? u32x4{pk[1][0], pk[1][1], pk[0][0], pk[0][1]} : u32x4{pk[0][0], pk[0][1], pk[1][0], pk[1][1]};
    ((u32x4*)(ws + WS_QF))[((tm * 8 + wm * 2 + ma) * 32 + head) * 64 + lane] = o;
  }
  {
    f32x4 sa[2][2];
#pragma unroll
    for (int m = 0; m < 2; ++m) {
      unsigned ra = lds0 + O_ROW + (wm * 32 + (m ^ mx) * 16 + 4 * g) * 8;
      sa[m][0] = *LDS3(f32x4, ra); sa[m][1] = *LDS3(f32x4, ra + 16);
    }
    bf16x8 fr[4];
#pragma unroll
    for (int j = 2; j < 6; ++j) {
      unsigned ca = lds0 + O_CS + (wn * 96 + (j ^ jx) * 16 + r15) * 4;
      float cs = *LDS3(float, ca) + *LDS3(float, ca + 768);
      u32x4 u;
#pragma unroll
      for (int m = 0; m < 2; ++m) {
        float v[4];
#pragma unroll
        for (int r = 0; r < 4; ++r) {
          float t = (acc[m][j][r] - sa[m][r >> 1][(r & 1) * 2] * cs) * sa[m][r >> 1][(r & 1) * 2 + 1];
          v[r] = j < 4 ? phi(t) : t;
        }
        u[m * 2] = pack_bf16(v[0], v[1]); u[m * 2 + 1] = pack_bf16(v[2], v[3]);
      }
      fr[j - 2] = __builtin_bit_cast(bf16x8, u);
    }
    bf16x8 ones = __builtin_bit_cast(bf16x8, u32x4{ONES2, ONES2, ONES2, ONES2});
    f32x4 zero = {0, 0, 0, 0};
#pragma unroll
    for (int fd = 0; fd < 2; ++fd) {
#pragma unroll
      for (int fe = 0; fe < 2; ++fe) {
        f32x4 kv = mfma16(fr[fd], fr[2 + fe], zero);
        *LDS3(f32x4, lds0 + O_RED + ((wave * 4 + ((fd ^ jx) * 2 + (fe ^ jx))) * 64 + lane) * 16) = kv;
      }
      f32x4 z = mfma16(fr[fd], ones, zero);
      if (r15 == 0) *LDS3(f32x4, lds0 + O_Z + ((wave * 2 + (fd ^ jx)) * 4 + g) * 16) = z;
    }
  }
  wait_lgkm0(); bar();
  {
    int w2 = tid >> 8, i = tid & 255;
    f32x4 s = *LDS3(f32x4, lds0 + O_RED + (w2 * 256 + i) * 16);
#pragma unroll
    for (int k = 1; k < 4; ++k) s += *LDS3(f32x4, lds0 + O_RED + ((k * 2 + w2) * 256 + i) * 16);
    ((f32x4*)(ws + WS_KVP))[((2 * tn + w2) * 16 + tm) * 256 + i] = s;
    if (tid < 16) {
      int w3 = tid >> 3, c = tid & 7;
      f32x4 z = *LDS3(f32x4, lds0 + O_Z + (w3 * 8 + c) * 16);
#pragma unroll
      for (int k = 1; k < 4; ++k) z += *LDS3(f32x4, lds0 + O_Z + ((k * 2 + w3) * 8 + c) * 16);
      ((f32x4*)(ws + WS_ZP))[((2 * tn + w3) * 16 + tm) * 8 + c] = z;
    }
  }
}

__global__ __launch_bounds__(256) void kB(const void* __restrict__ wg, const void* __restrict__ wu, char* __restrict__ ws) {
  __shared__ __attribute__((aligned(16))) char sm[4096];
  unsigned lds0 = (unsigned)(size_t)sm;
  int tid = threadIdx.x, lane = tid & 63, wave = tid >> 6, b = blockIdx.x;
  if (b < 256) {
    int h = (b & 7) * 4 + ((b >> 3) & 3), rg = b >> 5;
    const f32x4* kvp = (const f32x4*)(ws + WS_KVP) + h * 4096 + tid;
    f32x4 p[16];
#pragma unroll
    for (int i = 0; i < 16; ++i) p[i] = kvp[i * 256];
    f32x4 zs = {0, 0, 0, 0};
    if (tid < 8) {
      const f32x4* zp = (const f32x4*)(ws + WS_ZP) + h * 128 + tid;
#pragma unroll
      for (int i = 0; i < 16; ++i) zs += zp[i * 8];
    }
    u32x4 q[4];
#pragma unroll
    for (int i = 0; i < 4; ++i) q[i] = ((const u32x4*)(ws + WS_QF))[((rg * 16 + wave * 4 + i) * 32 + h) * 64 + lane];
    f32x4 s = ((p[0] + p[1]) + (p[2] + p[3])) + ((p[4] + p[5]) + (p[6] + p[7])) + (((p[8] + p[9]) + (p[10] + p[11])) + ((p[12] + p[13]) + (p[14] + p[15])));
    {
      int fd = wave >> 1, e = (wave & 1) * 16 + (lane & 15), g = lane >> 4;
      int fe = (e >> 2) & 1, i = ((e >> 3) << 2) | (e & 3);
      *LDS3(u32x2, lds0 + ((fe * 64 + g * 16 + i) * 2 + fd) * 8) = u32x2{pack_bf16(s[0], s[1]), pack_bf16(s[2], s[3])};
      if (tid < 8) *LDS3(u32x2, lds0 + 2048 + ((tid & 3) * 2 + (tid >> 2)) * 8) = u32x2{pack_bf16(zs[0], zs[1]), pack_bf16(zs[2], zs[3])};
    }
    __syncthreads();
    bf16x8 k0 = lds_b128(lds0 + lane * 16), k1 = lds_b128(lds0 + 1024 + lane * 16), zf = lds_b128(lds0 + 2048 + (lane >> 4) * 16);
    f32x4 zero = {0, 0, 0, 0};
#pragma unroll
    for (int i = 0; i < 4; ++i) {
      bf16x8 qq = __builtin_bit_cast(bf16x8, q[i]);
      f32x4 n0 = mfma16(k0, qq, zero), n1 = mfma16(k1, qq, zero), dn = mfma16(zf, qq, zero);
      float inv = 1.f / (dn[0] + 1e-6f);
      u32x4 o = {pack_bf16(n0[0] * inv, n0[1] * inv), pack_bf16(n0[2] * inv, n0[3] * inv), pack_bf16(n1[0] * inv, n1[1] * inv), pack_bf16(n1[2] * inv, n1[3] * inv)};
      ((u32x4*)(ws + WS_OT))[((rg * 16 + wave * 4 + i) * 128 + h * 4) * 16 + lane] = o;
    }
  } else {
    int cb = b - 256, rc = cb & 3, cs = cb >> 2, mat = cs / 56, col0 = (cs % 56) * 64;
    const char* w = (const char*)(mat ? wu : wg) + ((size_t)(rc * 256 + (tid >> 3)) * FF + col0 + (tid & 7) * 8) * 2;
    u32x4 v[8];
#pragma unroll
    for (int i = 0; i < 8; ++i) v[i] = *(const u32x4*)(w + (size_t)i * 32 * FF * 2);
    float a[8];
#pragma unroll
    for (int c = 0; c < 4; ++c) {
      float lo = 0, hi = 0;
#pragma unroll
      for (int i = 0; i < 8; ++i) { lo += bf_lo(v[i][c]); hi += bf_hi(v[i][c]); }
      a[2 * c] = lo; a[2 * c + 1] = hi;
    }
#pragma unroll
    for (int c = 0; c < 8; ++c) { a[c] = sum_g(a[c]); a[c] += __shfl_xor(a[c], 8); }
    if (lane < 8) {
      *LDS3(f32x4, lds0 + (wave * 64 + lane * 8) * 4) = f32x4{a[0], a[1], a[2], a[3]};
      *LDS3(f32x4, lds0 + (wave * 64 + lane * 8 + 4) * 4) = f32x4{a[4], a[5], a[6], a[7]};
    }
    __syncthreads();
    if (tid < 64) {
      float s = (*LDS3(float, lds0 + tid * 4) + *LDS3(float, lds0 + (64 + tid) * 4)) + (*LDS3(float, lds0 + (128 + tid) * 4) + *LDS3(float, lds0 + (192 + tid) * 4));
      ((float*)(ws + WS_CS))[rc * 2 * FF + mat * FF + col0 + tid] = s;
    }
  }
}

template <int NK> using CR = Cfg<128, 64, 64, 6, 4, 1, 2, NK, 0, 0>;
template <int MODE>
__global__ __launch_bounds__(512, 2) void kR(const void* __restrict__ w, const void* __restrict__ xres, void* __restrict__ out, char* __restrict__ ws) {
  constexpr int K = MODE ? FF : DM;
  using C = CR<K / 64>;
  __shared__ __attribute__((aligned(256))) char sm[C::LDS];
  unsigned lds0 = (unsigned)(size_t)sm;
  int tid = threadIdx.x, lane = tid & 63, wave = __builtin_amdgcn_readfirstlane(tid >> 6);
  int wk = wave & 1, wm = wave >> 1;
  int b = blockIdx.x, xcd = b & 7, idx = b >> 3;
  int tm = (xcd >> 1) * 4 + (idx & 3), tn = (xcd & 1) * 8 + (idx >> 2);
  int g = lane >> 4, r15 = lane & 15;
  int mf = tm * 8 + wm * 2 + wk, row = mf * 16 + r15;
  u32x4 res[2];
#pragma unroll
  for (int jp = 0; jp < 2; ++jp) {
    int c8 = tn * 8 + 2 * (jp * 2 + (g & 1)) + (g >> 1);
    if (MODE == 0) res[jp] = *(const u32x4*)((const char*)xres + ((size_t)row * DM + c8 * 8) * 2);
    else res[jp] = ((const u32x4*)(ws + WS_HT))[(mf * 128 + c8) * 16 + r15];
  }
  Loader<C> L;
  loader_init<C>(L, wave, lane, ws + (MODE ? WS_ACT : WS_OT), tm * 128, K, w, w, DM, 0, [&](int lc) { return tn * 64 + lc * 8; });
  prologue<C>(L, wave, lds0);
  RdAddr<C> R; rd_init<C>(R, lds0, wm, 0, wk, lane, wk, 0, 0);
  f32x4 acc[2][4];
#pragma unroll
  for (int m = 0; m < 2; ++m)
#pragma unroll
    for (int j = 0; j < 4; ++j) acc[m][j] = f32x4{0, 0, 0, 0};
  NoHook hk;
  mainloop<C, 0xfu>(acc, L, R, wave, lds0, hk);
#pragma unroll
  for (int j = 0; j < 4; ++j) *LDS3(f32x4, lds0 + ((wave * 4 + j) * 64 + lane) * 16) = acc[1][j];
  wait_lgkm0(); bar();
#pragma unroll
  for (int j = 0; j < 4; ++j) acc[0][j] += *LDS3(f32x4, lds0 + (((wave ^ 1) * 4 + j) * 64 + lane) * 16);
  float ss = 0, sq = 0;
#pragma unroll
  for (int jp = 0; jp < 2; ++jp) {
    f32x4 a = acc[0][2 * jp], c = acc[0][2 * jp + 1];
#pragma unroll
    for (int r = 0; r < 4; ++r) { float u = a[r], v = c[r]; swap16(u, v); a[r] = u; c[r] = v; }
    u32x4 o;
    o[0] = pack_bf16(a[0] + bf_lo(res[jp][0]), a[1] + bf_hi(res[jp][0]));
    o[1] = pack_bf16(a[2] + bf_lo(res[jp][1]), a[3] + bf_hi(res[jp][1]));
    o[2] = pack_bf16(c[0] + bf_lo(res[jp][2]), c[1] + bf_hi(res[jp][2]));
    o[3] = pack_bf16(c[2] + bf_lo(res[jp][3]), c[3] + bf_hi(res[jp][3]));
    int c8 = tn * 8 + 2 * (jp * 2 + (g & 1)) + (g >> 1);
    if (MODE == 0) {
      ((u32x4*)(ws + WS_HT))[(mf * 128 + c8) * 16 + r15] = o;
#pragma unroll
      for (int i = 0; i < 4; ++i) { ss = dot2(o[i], ONES2, ss); sq = dot2(o[i], o[i], sq); }
    } else {
      *(u32x4*)((char*)out + ((size_t)row * DM + c8 * 8) * 2) = o;
    }
  }
  if (MODE == 0) {
    ss = sum_g(ss); sq = sum_g(sq);
    if (g == 0) ((f32x2*)(ws + WS_ST))[row * 16 + tn] = f32x2{ss, sq};
  }
}

using CD = Cfg<256, 224, 32, 4, 4, 2, 1, 32, 0, 1>;
DEVI f32x4 gload4(const void* p) { f32x4 r; asm volatile("global_load_dwordx4 %0, %1, off" : "=v"(r) : "v"(p) : "memory"); return r; }
DEVI float gload1(const void* p) { float r; asm volatile("global_load_dword %0, %1, off" : "=v"(r) : "v"(p) : "memory"); return r; }
DEVI float geglu(float x, float u) {
  constexpr float K1 = -2.f * 0.7978845608028654f * 1.4426950408889634f, K2 = K1 * 0.044715f;
  float e = __builtin_amdgcn_exp2f(x * (K1 + K2 * x * x));
  return x * __builtin_amdgcn_rcpf(1.f + e) * u;
}
__global__ __launch_bounds__(512, 2) void kD(const void* __restrict__ wg, const void* __restrict__ wu, char* __restrict__ ws) {
  using C = CD;
  __shared__ __attribute__((aligned(256))) char sm[C::LDS + 4096];
  unsigned lds0 = (unsigned)(size_t)sm;
  constexpr unsigned O_ROW = C::LDS, O_CS = C::LDS + 2048;
  int tid = threadIdx.x, lane = tid & 63, wave = __builtin_amdgcn_readfirstlane(tid >> 6);
  int wn = wave & 1, wm = wave >> 1;
  int b = blockIdx.x, xcd = b & 7, idx = b >> 3;
  int tm = (xcd >> 2) * 4 + (idx & 3), tn = (xcd & 3) * 8 + (idx >> 2);
  {
    const char* sp = ws + WS_ST + (size_t)(tm * 256) * 128 + tid * 64;
    f32x4 s0 = gload4(sp), s1 = gload4(sp + 16), s2 = gload4(sp + 32), s3 = gload4(sp + 48);
    int ct = tid < 224 ? tid : 223;
    const float* cp = (const float*)(ws + WS_CS) + (ct < 112 ? tn * 112 + ct : FF + tn * 112 + ct - 112);
    float c0 = gload1(cp), c1 = gload1(cp + 2 * FF), c2 = gload1(cp + 4 * FF), c3 = gload1(cp + 6 * FF);
    Loader<C> L;
    loader_init<C>(L, wave, lane, ws + WS_HT, tm * 256, DM, wg, wu, FF, 0, [&](int lc) { return tn * 112 + lc * 8; });
    prologue<C>(L, wave, lds0);
    wait_stages<C, C::S>(wave);
    asm volatile("" : "+v"(s0), "+v"(s1), "+v"(s2), "+v"(s3), "+v"(c0), "+v"(c1), "+v"(c2), "+v"(c3));
    f32x4 t = (s0 + s1) + (s2 + s3);
    float s = t[0] + t[2], q = t[1] + t[3];
    s += __shfl_xor(s, 1); q += __shfl_xor(q, 1);
    float mean = s * (1.f / DM), var = fmaxf(q * (1.f / DM) - mean * mean, 0.f);
    float rstd = __builtin_amdgcn_rsqf(var + 1e-5f);
    if (!(tid & 1)) *LDS3(f32x2, lds0 + O_ROW + (tid >> 1) * 8) = f32x2{mean, rstd};
    if (tid < 224) *LDS3(float, lds0 + O_CS + tid * 4) = (c0 + c1) + (c2 + c3);
    RdAddr<C> R; rd_init<C>(R, lds0, wm, wn, 0, lane, 0, 0, 0);
    f32x4 acc[4][7];
#pragma unroll
    for (int m = 0; m < 4; ++m)
#pragma unroll
      for (int j = 0; j < 7; ++j) acc[m][j] = f32x4{0, 0, 0, 0};
    NoHook hk;
    mainloop<C, 0x7fu>(acc, L, R, wave, lds0, hk);
    int g = lane >> 4, r15 = lane & 15, hi = g >> 1;
    f32x2 st[2];
#pragma unroll
    for (int p = 0; p < 2; ++p) st[p] = *LDS3(f32x2, lds0 + O_ROW + (wm * 64 + (p * 2 + hi) * 16 + r15) * 8);
    int smf = tm * 16 + wm * 4 + ((g & 1) * 2 + hi);
    u32x4* dst = (u32x4*)(ws + WS_ACT) + ((size_t)smf * (FF / 8) + tn * 14 + wn * 7) * 16 + r15;
#pragma unroll
    for (int j = 0; j < 7; ++j) {
      unsigned ca = lds0 + O_CS + ((wn * 7 + j) * 8 + (g & 1) * 4) * 4;
      f32x4 cg = *LDS3(f32x4, ca), cu = *LDS3(f32x4, ca + 448);
      unsigned pk[2][2];
#pragma unroll
      for (int p = 0; p < 2; ++p) {
        float a[4];
#pragma unroll
        for (int r = 0; r < 4; ++r) {
          float x = acc[2 * p][j][r], y = acc[2 * p + 1][j][r];
          swap32(x, y);
          a[r] = geglu((x - st[p][0] * cg[r]) * st[p][1], (y - st[p][0] * cu[r]) * st[p][1]);
        }
        pk[p][0] = pack_bf16(a[0], a[1]); pk[p][1] = pack_bf16(a[2], a[3]);
      }
      swap16u(pk[0][0], pk[1][0]); swap16u(pk[0][1], pk[1][1]);
      dst[j * 16] = u32x4{pk[0][0], pk[0][1], pk[1][0], pk[1][1]};
    }
  }
}

extern "C" void kernel_launch(void* const* d_in, const int* in_sizes, int n_in, void* d_out, int out_size, void* d_ws, size_t ws_size,
                              hipStream_t stream) {
  char* ws = (char*)d_ws;
  kA<<<256, 512, 0, stream>>>(d_in[0], d_in[1], ws);
  kB<<<256 + 448, 256, 0, stream>>>(d_in[4], d_in[5], ws);
  kR<0><<<256, 512, 0, stream>>>(d_in[2], d_in[0], nullptr, ws);
  kD<<<256, 512, 0, stream>>>(d_in[4], d_in[5], ws);
  kR<1><<<256, 512, 0, stream>>>(d_in[6], nullptr, d_out, ws);
}
```

```cpp
#include <hip/hip_runtime.h>
#include <stdint.h>

typedef __attribute__((ext_vector_type(4))) short s16x4;
typedef __attribute__((ext_vector_type(8))) short s16x8;
typedef __attribute__((ext_vector_type(8))) __bf16 bf16x8;
typedef __attribute__((ext_vector_type(2))) __bf16 bf16x2;
typedef __attribute__((ext_vector_type(4))) float f32x4;
typedef __attribute__((ext_vector_type(2))) float f32x2;
typedef __attribute__((ext_vector_type(4))) unsigned u32x4;
typedef __attribute__((ext_vector_type(2))) unsigned u32x2;
#define LDS3(T, a) ((__attribute__((address_space(3))) T*)(size_t)(a))
#define DEVI __device__ __forceinline__

template <int N> DEVI void wait_vm() { asm volatile("s_waitcnt vmcnt(%0)" ::"n"(N) : "memory"); }
DEVI void wait_lgkm0() { asm volatile("s_waitcnt lgkmcnt(0)" ::: "memory"); }
DEVI void bar() { __builtin_amdgcn_s_barrier(); }
DEVI void glds16(unsigned voff, const char* sbase, unsigned lds) {
  asm volatile("s_mov_b32 m0, %2\n\ts_nop 0\n\tglobal_load_lds_dwordx4 %0, %1" ::"v"(voff), "s"(sbase), "s"(lds) : "memory");
}
DEVI bf16x8 lds_b128(unsigned a) { return *LDS3(bf16x8, a); }
DEVI s16x4 lds_tr(unsigned a) { return __builtin_amdgcn_ds_read_tr16_b64_v4i16(LDS3(s16x4, a)); }
DEVI unsigned pack_bf16(float lo, float hi) {
  bf16x2 v; v[0] = (__bf16)lo; v[1] = (__bf16)hi; return __builtin_bit_cast(unsigned, v);
}
DEVI float bf_lo(unsigned u) { return __uint_as_float(u << 16); }
DEVI float bf_hi(unsigned u) { return __uint_as_float(u & 0xffff0000u); }
DEVI float dot2(unsigned a, unsigned b, float c) {
  return __builtin_amdgcn_fdot2_f32_bf16(__builtin_bit_cast(bf16x2, a), __builtin_bit_cast(bf16x2, b), c, false);
}
DEVI void swap16(float& a, float& b) {
  auto r = __builtin_amdgcn_permlane16_swap(__float_as_uint(a), __float_as_uint(b), false, false);
  a = __uint_as_float(r[0]); b = __uint_as_float(r[1]);
}
DEVI void swap16u(unsigned& a, unsigned& b) {
  auto r = __builtin_amdgcn_permlane16_swap(a, b, false, false);
  a = r[0]; b = r[1];
}
DEVI void swap32(float& a, float& b) {
  auto r = __builtin_amdgcn_permlane32_swap(__float_as_uint(a), __float_as_uint(b), false, false);
  a = __uint_as_float(r[0]); b = __uint_as_float(r[1]);
}
DEVI float sum_g(float x) {
  float a = x, b = x; swap16(a, b); x = a + b;
  a = x; b = x; swap32(a, b); return a + b;
}
DEVI f32x4 mfma16(bf16x8 a, bf16x8 b, f32x4 c) { return __builtin_amdgcn_mfma_f32_16x16x32_bf16(a, b, c, 0, 0, 0); }

template <int BM_, int BN_, int BK_, int S_, int WM_, int WN_, int WK_, int NK_, int AMODE_, int BMODE_, int NPF_ = 0>
struct Cfg {
  static constexpr int NPF = NPF_;
  static constexpr int BM = BM_, BN = BN_, BK = BK_, S = S_, WM = WM_, WN = WN_, WK = WK_, NK = NK_, AMODE = AMODE_, BMODE = BMODE_;
  static constexpr int NW = WM * WN * WK;
  static constexpr int MF = BM / WM / 16, NF = BN / WN / 16, KF = BK / 32, KFW = KF / WK;
  static constexpr int A_BYTES = BM * BK * 2;
  static constexpr int B_BYTES = BN * BK * 2 + (BMODE == 1 ? 256 : 0);
  static constexpr int STAGE = A_BYTES + B_BYTES;
  static constexpr int PA = A_BYTES / 1024, PB = BN * BK * 2 / 1024, P = PA + PB;
  static constexpr int NLW = NPF ? NW - 1 : NW;
  static constexpr int NP = (P + NLW - 1) / NLW;
  static constexpr int FULLW = P - (NP - 1) * NLW;
  static constexpr int RS = (BMODE == 1 ? BN : BN * 2);
  static constexpr int LDS = S * STAGE;
  static constexpr bool SLOT = (MF == 2);
};

template <class C> struct Frags { bf16x8 a[C::KFW][C::MF]; bf16x8 b[C::KFW][C::NF]; };
template <class C> struct Loader {
  const char* sbase[C::NP]; unsigned voff[C::NP]; unsigned ldso[C::NP]; unsigned sstr[C::NP];
  const char* pfb[4]; unsigned pfv, pfl, pfs[4];
};
DEVI void glds4(unsigned voff, const char* sbase, unsigned lds) {
  asm volatile("s_mov_b32 m0, %2\n\ts_nop 0\n\tglobal_load_lds_dword %0, %1" ::"v"(voff), "s"(sbase), "s"(lds) : "memory");
}
template <class C> DEVI void issue_pf(Loader<C>& L, int i) { glds4(L.pfv, L.pfb[i], L.pfl); L.pfb[i] += L.pfs[i]; }
DEVI const char* uni_ptr(const char* p) {
  unsigned long long u = (unsigned long long)p;
  unsigned lo = __builtin_amdgcn_readfirstlane((unsigned)u), hi = __builtin_amdgcn_readfirstlane((unsigned)(u >> 32));
  return (const char*)(((unsigned long long)hi << 32) | lo);
}

template <class C, class ColMap>
DEVI void loader_init(Loader<C>& L, int wave, int lane, const void* A, int m0, int lda, const void* B0, const void* B1, int ldb, int k0,
                      ColMap colmap) {
#pragma unroll
  for (int i = 0; i < C::NP; ++i) {
    int p = wave + i * C::NLW;
    if (p >= C::P) p = C::P - 1;
    if (p < C::PA) {
      if (C::AMODE == 0) {
        int mb = p / C::KF, kb = p % C::KF;
        L.sbase[i] = (const char*)A + ((size_t)(m0 / 16 + mb) * (lda / 8) + (k0 / 8 + kb * 4)) * 256;
        L.voff[i] = lane * 16;
        L.sstr[i] = C::BK / 8 * 256;
      } else {
        int row = 8 * p + (lane >> 3), pos = lane & 7, lc = pos ^ ((row >> 1) & 7);
        L.sbase[i] = (const char*)A + ((size_t)(m0 + 8 * p) * lda + k0) * 2;
        L.voff[i] = ((lane >> 3) * lda + lc * 8) * 2;
        L.sstr[i] = C::BK * 2;
      }
      L.ldso[i] = p * 1024;
    } else {
      int pb = p - C::PA;
      if (C::BMODE == 0) {
        constexpr int CH = C::BN / 8;
        int ci = pb * 64 + lane, k = ci / CH, c = ci % CH, u = c >> 1;
        int sg = (C::RS % 256 == 0) ? ((k & 3) | (((k >> 3) & 1) << 2)) : (C::RS % 256 == 128) ? (((k >> 1) & 1) | (((k >> 3) & 1) << 1)) : ((k >> 3) & 1);
        int lc = ((u ^ sg) << 1) | (c & 1);
        L.sbase[i] = (const char*)B0 + (size_t)k0 * ldb * 2;
        L.voff[i] = (k * ldb + colmap(lc)) * 2;
        L.ldso[i] = C::A_BYTES + pb * 1024;
      } else {
        constexpr int CH = C::BN / 16, HP = C::PB / 2;
        int half = pb / HP, pp = pb % HP;
        int ci = pp * 64 + lane, k = ci / CH, c = ci % CH, lc = c ^ ((k >> 3) & 1);
        L.sbase[i] = (const char*)(half ? B1 : B0) + (size_t)k0 * ldb * 2;
        L.voff[i] = (k * ldb + colmap(lc)) * 2;
        L.ldso[i] = C::A_BYTES + half * (HP * 1024 + 128) + pp * 1024;
      }
      L.sstr[i] = C::BK * ldb * 2;
    }
    {
      unsigned long long u = (unsigned long long)L.sbase[i];
      unsigned lo = __builtin_amdgcn_readfirstlane((unsigned)u), hi = __builtin_amdgcn_readfirstlane((unsigned)(u >> 32));
      L.sbase[i] = (const char*)(((unsigned long long)hi << 32) | lo);
      L.ldso[i] = __builtin_amdgcn_readfirstlane(L.ldso[i]);
      L.sstr[i] = __builtin_amdgcn_readfirstlane(L.sstr[i]);
    }
  }
}

template <class C> DEVI void issue_piece(Loader<C>& L, int i, int wave, unsigned bufbase) {
  if (i < C::NP - 1 ? (C::NLW == C::NW || wave < C::NLW) : (C::FULLW == C::NW || wave < C::FULLW)) glds16(L.voff[i], L.sbase[i], bufbase + L.ldso[i]);
  L.sbase[i] += L.sstr[i];
}
template <class C, int NST, int X = 0> DEVI void wait_stages(int wave) {
  if (C::FULLW == C::NW || wave < C::FULLW) wait_vm<NST * C::NP + X>();
  else if (C::NLW == C::NW || wave < C::NLW) wait_vm<NST*(C::NP - 1) + X>();
}

template <class C> struct RdAddr { unsigned a[C::KFW][2]; unsigned b[C::NF]; unsigned kbo[2]; };
template <class C> DEVI void rd_init(RdAddr<C>& R, unsigned lds0, int wm, int wn, int wk, int lane, int mx, int kx, int jx) {
  int g = lane >> 4, r15 = lane & 15, q = (lane & 15) >> 2, p = lane & 3;
#pragma unroll
  for (int kb = 0; kb < C::KFW; ++kb)
#pragma unroll
    for (int m = 0; m < 2; ++m) {
      int ka = wk * C::KFW + (kb ^ kx), ma = wm * C::MF + (m ^ mx);
      if (C::AMODE == 0) R.a[kb][m] = lds0 + (ma * C::KF + ka) * 1024 + lane * 16;
      else R.a[kb][m] = lds0 + (ma * 16 + r15) * 128 + (((ka * 4 + g) ^ ((r15 >> 1) & 7)) * 16);
    }
  R.kbo[0] = kx * 32 * C::RS; R.kbo[1] = (1 ^ kx) * 32 * C::RS;
  int krow = wk * C::KFW * 32 + 8 * g + q;
#pragma unroll
  for (int j = 0; j < C::NF; ++j) {
    int U = wn * C::NF + (j ^ jx);
    if (C::BMODE == 0) {
      int sg = (C::RS % 256 == 0) ? (q | ((g & 1) << 2)) : (C::RS % 256 == 128) ? ((q >> 1) | ((g & 1) << 1)) : (g & 1);
      R.b[j] = lds0 + C::A_BYTES + krow * C::RS + ((U ^ sg) * 32) + p * 8;
    } else {
      R.b[j] = lds0 + C::A_BYTES + (p >> 1) * (C::PB / 2 * 1024 + 128) + krow * C::RS + ((U ^ (g & 1)) * 16) + (p & 1) * 8;
    }
  }
}
template <class C> DEVI void read_a(Frags<C>& f, const RdAddr<C>& R, unsigned so, int kb, int mf) {
  if (C::SLOT) f.a[kb][mf] = lds_b128(R.a[kb][mf & 1] + so);
  else f.a[kb][mf] = lds_b128(R.a[0][0] + so + (mf * C::KF + kb) * 1024);
}
template <class C> DEVI void read_b(Frags<C>& f, const RdAddr<C>& R, unsigned so, int kb, int j) {
  unsigned a = (C::AMODE == 1) ? R.b[j] + (so + R.kbo[kb & 1]) : R.b[j] + so + (kb * 32) * C::RS;
  s16x4 lo = lds_tr(a);
  s16x4 hi = lds_tr(a + 4 * C::RS);
  s16x8 v = __builtin_shufflevector(lo, hi, 0, 1, 2, 3, 4, 5, 6, 7);
  f.b[kb][j] = __builtin_bit_cast(bf16x8, v);
}
template <class C> DEVI void read_all(Frags<C>& f, const RdAddr<C>& R, unsigned so) {
#pragma unroll
  for (int kb = 0; kb < C::KFW; ++kb) {
#pragma unroll
    for (int m = 0; m < C::MF; ++m) read_a<C>(f, R, so, kb, m);
#pragma unroll
    for (int j = 0; j < C::NF; ++j) read_b<C>(f, R, so, kb, j);
  }
}

template <class C, unsigned SWAPMASK, bool ISSUE, bool READ, int NOUT, int PF, class Hook>
DEVI void iter(f32x4 (&acc)[C::MF][C::NF], const Frags<C>& cur, Frags<C>& nxt, Loader<C>& L, const RdAddr<C>& R, int wave, unsigned lds0,
               unsigned& bcur, Hook& hook) {
  wait_lgkm0();
  if (READ) { wait_stages<C, NOUT>(wave); }
  bar();
  if (PF == 9) hook.tail();
  unsigned bn = bcur + C::STAGE; if (bn == C::LDS) bn = 0;
  constexpr int NM = C::KFW * C::MF * C::NF;
  constexpr int NR = C::KFW * (C::MF + C::NF);
  constexpr int SEG = C::NP + 1;
#pragma unroll
  for (int s = 0; s < SEG; ++s) {
    if (READ) {
#pragma unroll
      for (int r = s * NR / SEG; r < (s + 1) * NR / SEG; ++r) {
        int kb = r / (C::MF + C::NF), x = r % (C::MF + C::NF);
        if (x < C::MF) read_a<C>(nxt, R, bn, kb, x); else read_b<C>(nxt, R, bn, kb, x - C::MF);
      }
    }
#pragma unroll
    for (int i = s * NM / SEG; i < (s + 1) * NM / SEG; ++i) {
      int kb = i / (C::MF * C::NF), x = i % (C::MF * C::NF), j = x / C::MF, m = x % C::MF;
      if ((SWAPMASK >> j) & 1) acc[m][j] = mfma16(cur.b[kb][j], cur.a[kb][m], acc[m][j]);
      else acc[m][j] = mfma16(cur.a[kb][m], cur.b[kb][j], acc[m][j]);
    }
    hook.seg(cur, s, SEG);
#pragma unroll
    for (int x = 0; x < 8; ++x) {
      __builtin_amdgcn_sched_group_barrier(0x8, 1, 0);
      __builtin_amdgcn_sched_group_barrier(0x100, 1, 0);
      if (Hook::VALU) __builtin_amdgcn_sched_group_barrier(0x2, Hook::VALU, 0);
    }
    __builtin_amdgcn_sched_barrier(0);
    if (PF && PF < 9 && (s == 1 || s == 3) && (PF - 1) * 2 + (s >> 1) < C::NPF) { if (wave == C::NW - 1) issue_pf<C>(L, (PF - 1) * 2 + (s >> 1)); }
    if (ISSUE && s < C::NP) { issue_piece<C>(L, s, wave, lds0 + bcur); __builtin_amdgcn_sched_barrier(0); }
  }
  bcur = bn;
}

template <class C> DEVI void prologue(Loader<C>& L, int wave, unsigned lds0) {
#pragma unroll
  for (int s = 0; s < C::S; ++s) {
#pragma unroll
    for (int i = 0; i < C::NP; ++i) issue_piece<C>(L, i, wave, lds0 + s * C::STAGE);
  }
}

template <class C, unsigned SWAPMASK, class Hook>
DEVI void mainloop(f32x4 (&acc)[C::MF][C::NF], Loader<C>& L, const RdAddr<C>& R, int wave, unsigned lds0, Hook& hook) {
  constexpr int S = C::S, NK = C::NK;
  Frags<C> f0, f1;
  wait_stages<C, S - 1>(wave);
  bar();
  read_all<C>(f0, R, 0);
  unsigned bcur = 0;
  constexpr int NMAIN = NK - S;
  constexpr int NPAIR = NMAIN / 2;
  for (int it = 0; it < NPAIR; ++it) {
    iter<C, SWAPMASK, true, true, S - 2, (C::NPF > 0 ? 1 : 0)>(acc, f0, f1, L, R, wave, lds0, bcur, hook);
    iter<C, SWAPMASK, true, true, S - 2, (C::NPF > 2 ? 2 : 0)>(acc, f1, f0, L, R, wave, lds0, bcur, hook);
  }
  constexpr int T0 = 2 * NPAIR;
#define TAILIT(T, CUR, NXT)                                                        \
  if constexpr (T0 + (T) < NK) {                                                   \
    constexpr int kt = T0 + (T);                                                   \
    constexpr bool iss = kt + S < NK, rd = kt + 1 < NK;                            \
    constexpr int last_issued = (kt + S - 1 < NK - 1) ? kt + S - 1 : NK - 1;       \
    constexpr int nout = rd ? last_issued - (kt + 1) : 0;                          \
    iter<C, SWAPMASK, iss, rd, nout, (kt == NK - 2 ? 9 : 0)>(acc, CUR, NXT, L, R, wave, lds0, bcur, hook); \
  }
  TAILIT(0, f0, f1) TAILIT(1, f1, f0) TAILIT(2, f0, f1) TAILIT(3, f1, f0) TAILIT(4, f0, f1) TAILIT(5, f1, f0) TAILIT(6, f0, f1) TAILIT(7, f1, f0)
#undef TAILIT
}
struct NoHook { static constexpr int VALU = 0; template <class F> DEVI void seg(const F&, int, int) const {} DEVI void tail() const {} };
template <int N> struct PfHook {
  static constexpr int VALU = 0;
  const char* p; unsigned v, l;
  template <class F> DEVI void seg(const F&, int, int) const {}
  DEVI void tail() const {
#pragma unroll
    for (int i = 0; i < N; ++i) glds4(v, p + i * 4096, l);
  }
};

#define PFV 1
constexpr int SEQ = 2048, DM = 1024, FF = 3584;
constexpr size_t MB = 1u << 20;
constexpr size_t WS_QF = 0;
constexpr size_t WS_OT = 4 * MB;
constexpr size_t WS_HT = 8 * MB;
constexpr size_t WS_KVP = 12 * MB;
constexpr size_t WS_ZP = 14 * MB;
constexpr size_t WS_ST = 14 * MB + 256 * 1024;
constexpr size_t WS_CS = 14 * MB + 768 * 1024;
constexpr size_t WS_ACT = 16 * MB;
#define STAMP(kid, i)
#define STAMPW(kid, i)
constexpr unsigned ONES2 = 0x3f803f80u;

DEVI float phi(float t) { return (t > 0.f ? t + 1.f : __expf(t)) + 1e-6f; }

using CA = Cfg<128, 192, 64, 3, 4, 2, 1, 16, 1, 0, 0>;
struct HookA {
  static constexpr int VALU = 0;
  f32x4 rs, rq, c[3];
  const char* p; unsigned v, l;
  DEVI void tail() const {
    glds4(v, p, l); glds4(v, p + 4096, l);
  }
  template <class F> DEVI void seg(const F& f, int s, int SEG) {
    bf16x8 ones = __builtin_bit_cast(bf16x8, u32x4{ONES2, ONES2, ONES2, ONES2});
#pragma unroll
    for (int u = s * 7 / SEG; u < (s + 1) * 7 / SEG; ++u) {
      if (u == 0) rs = mfma16(ones, f.a[0][0], rs);
      if (u == 1) rq = mfma16(f.a[0][0], f.a[0][0], rq);
      if (u == 2) c[0] = mfma16(ones, f.b[0][0], c[0]);
      if (u == 3) c[1] = mfma16(ones, f.b[0][2], c[1]);
      if (u == 4) c[2] = mfma16(ones, f.b[0][4], c[2]);
      if (u == 5) rs = mfma16(ones, f.a[1][0], rs);
      if (u == 6) rq = mfma16(f.a[1][0], f.a[1][0], rq);
    }
  }
};

__global__ __launch_bounds__(512, 2) void kA(const void* __restrict__ x, const void* __restrict__ wqkv, const void* __restrict__ wo, const void* __restrict__ wg,
                                             const void* __restrict__ wu, const void* __restrict__ wd, char* __restrict__ ws) {
  using C = CA;
  STAMP(0, 0);
  __shared__ __attribute__((aligned(256))) char sm[C::LDS + 256];
  unsigned lds0 = (unsigned)(size_t)sm;
  int tid = threadIdx.x, lane = tid & 63, wave = __builtin_amdgcn_readfirstlane(tid >> 6);
  int wn = wave & 1, wm = wave >> 1;
  int b = blockIdx.x, xcd = b & 7, idx = b >> 3;
  int tm = (xcd >> 1) * 4 + (idx & 3), tn = (xcd & 1) * 8 + (idx >> 2);
  Loader<C> L;
  loader_init<C>(L, wave, lane, x, tm * 128, DM, wqkv, wqkv, 3 * DM, 0, [&](int lc) {
    int c = lc * 8, w = c / 96, r = c % 96;
    return (r >> 5) * DM + (2 * tn + w) * 32 + (r & 31);
  });
  prologue<C>(L, wave, lds0);
  int mx = wn, kx = wm & 1, jx = wm >> 1;
  RdAddr<C> R; rd_init<C>(R, lds0, wm, wn, 0, lane, mx, kx, jx);
  f32x4 acc[2][6];
#pragma unroll
  for (int m = 0; m < 2; ++m)
#pragma unroll
    for (int j = 0; j < 6; ++j) acc[m][j] = f32x4{0, 0, 0, 0};
  HookA hk; hk.v = lane * 64; hk.l = lds0 + C::LDS;
  {
    int s0 = wave * 2;
    const char* pp = s0 < 7 ? (const char*)wg + (size_t)b * 28672 + (s0 < 6 ? s0 : 5) * 4096 : s0 < 14 ? (const char*)wu + (size_t)b * 28672 + (s0 - 7) * 4096 : (const char*)wo + (size_t)b * 8192;
    hk.p = uni_ptr(pp);
    if (wave == 3) glds4(hk.v, uni_ptr((const char*)wu + (size_t)b * 28672), hk.l);
  }
  hk.rs = hk.rq = hk.c[0] = hk.c[1] = hk.c[2] = f32x4{0, 0, 0, 0};
  mainloop<C, 0x3u>(acc, L, R, wave, lds0, hk);
  STAMP(0, 1);

  int g = lane >> 4, r15 = lane & 15;
  constexpr unsigned O_ROW = 0, O_CS = 1024, O_Z = 4096, O_RED = 8192;
  {
    int rr = r15 & 3;
    float s = hk.rs[0], q = rr == 0 ? hk.rq[0] : rr == 1 ? hk.rq[1] : rr == 2 ? hk.rq[2] : hk.rq[3];
    float mean = s * (1.f / DM), var = fmaxf(q * (1.f / DM) - mean * mean, 0.f);
    float rstd = __builtin_amdgcn_rsqf(var + 1e-5f);
    if (g == (r15 >> 2)) *LDS3(f32x2, lds0 + O_ROW + (wm * 32 + wn * 16 + r15) * 8) = f32x2{mean, rstd};
    float cc = g == 1 ? hk.c[0][0] : g == 2 ? hk.c[1][0] : hk.c[2][0];
    if (g) *LDS3(float, lds0 + O_CS + (kx * 192 + wn * 96 + (((g - 1) * 2) ^ jx) * 16 + r15) * 4) = cc;
  }
  wait_lgkm0(); bar();
  int head = 2 * tn + wn;
#pragma unroll
  for (int m = 0; m < 2; ++m) {
    int ma = m ^ mx;
    f32x2 st = *LDS3(f32x2, lds0 + O_ROW + (wm * 32 + ma * 16 + r15) * 8);
    unsigned pk[2][2];
#pragma unroll
    for (int j = 0; j < 2; ++j) {
      unsigned ca = lds0 + O_CS + (wn * 96 + (j ^ jx) * 16 + 4 * g) * 4;
      f32x4 cs = *LDS3(f32x4, ca) + *LDS3(f32x4, ca + 768);
      float v[4];
#pragma unroll
      for (int r = 0; r < 4; ++r) v[r] = phi((acc[m][j][r] - st[0] * cs[r]) * st[1]);
      pk[j][0] = pack_bf16(v[0], v[1]); pk[j][1] = pack_bf16(v[2], v[3]);
    }
    u32x4 o = jx ? u32x4{pk[1][0], pk[1][1], pk[0][0], pk[0][1]} : u32x4{pk[0][0], pk[0][1], pk[1][0], pk[1][1]};
    ((u32x4*)(ws + WS_QF))[((tm * 8 + wm * 2 + ma) * 32 + head) * 64 + lane] = o;
  }
  {
    f32x4 sa[2][2];
#pragma unroll
    for (int m = 0; m < 2; ++m) {
      unsigned ra = lds0 + O_ROW + (wm * 32 + (m ^ mx) * 16 + 4 * g) * 8;
      sa[m][0] = *LDS3(f32x4, ra); sa[m][1] = *LDS3(f32x4, ra + 16);
    }
    bf16x8 fr[4];
#pragma unroll
    for (int j = 2; j < 6; ++j) {
      unsigned ca = lds0 + O_CS + (wn * 96 + (j ^ jx) * 16 + r15) * 4;
      float cs = *LDS3(float, ca) + *LDS3(float, ca + 768);
      u32x4 u;
#pragma unroll
      for (int m = 0; m < 2; ++m) {
        float v[4];
#pragma unroll
        for (int r = 0; r < 4; ++r) {
          float t = (acc[m][j][r] - sa[m][r >> 1][(r & 1) * 2] * cs) * sa[m][r >> 1][(r & 1) * 2 + 1];
          v[r] = j < 4 ? phi(t) : t;
        }
        u[m * 2] = pack_bf16(v[0], v[1]); u[m * 2 + 1] = pack_bf16(v[2], v[3]);
      }
      fr[j - 2] = __builtin_bit_cast(bf16x8, u);
    }
    bf16x8 ones = __builtin_bit_cast(bf16x8, u32x4{ONES2, ONES2, ONES2, ONES2});
    f32x4 zero = {0, 0, 0, 0};
#pragma unroll
    for (int fd = 0; fd < 2; ++fd) {
#pragma unroll
      for (int fe = 0; fe < 2; ++fe) {
        f32x4 kv = mfma16(fr[fd], fr[2 + fe], zero);
        *LDS3(f32x4, lds0 + O_RED + ((wave * 4 + ((fd ^ jx) * 2 + (fe ^ jx))) * 64 + lane) * 16) = kv;
      }
      f32x4 z = mfma16(fr[fd], ones, zero);
      if (r15 == 0) *LDS3(f32x4, lds0 + O_Z + ((wave * 2 + (fd ^ jx)) * 4 + g) * 16) = z;
    }
  }
  wait_lgkm0(); bar();
  {
    int w2 = tid >> 8, i = tid & 255;
    f32x4 s = *LDS3(f32x4, lds0 + O_RED + (w2 * 256 + i) * 16);
#pragma unroll
    for (int k = 1; k < 4; ++k) s += *LDS3(f32x4, lds0 + O_RED + ((k * 2 + w2) * 256 + i) * 16);
    ((f32x4*)(ws + WS_KVP))[((2 * tn + w2) * 16 + tm) * 256 + i] = s;
    if (tid < 16) {
      int w3 = tid >> 3, c = tid & 7;
      f32x4 z = *LDS3(f32x4, lds0 + O_Z + (w3 * 8 + c) * 16);
#pragma unroll
      for (int k = 1; k < 4; ++k) z += *LDS3(f32x4, lds0 + O_Z + ((k * 2 + w3) * 8 + c) * 16);
      ((f32x4*)(ws + WS_ZP))[((2 * tn + w3) * 16 + tm) * 8 + c] = z;
    }
  }
  STAMPW(0, 2);
}

__global__ __launch_bounds__(256) void kB(const void* __restrict__ wg, const void* __restrict__ wu, char* __restrict__ ws) {
  __shared__ __attribute__((aligned(16))) char sm[4096];
  unsigned lds0 = (unsigned)(size_t)sm;
  int tid = threadIdx.x, lane = tid & 63, wave = tid >> 6, b = blockIdx.x;
  STAMP(1, 0); STAMP(1, 1);
  if (b < 256) {
    int h = (b & 7) * 4 + ((b >> 3) & 3), rg = b >> 5;
    const f32x4* kvp = (const f32x4*)(ws + WS_KVP) + h * 4096 + tid;
    f32x4 p[16];
#pragma unroll
    for (int i = 0; i < 16; ++i) p[i] = kvp[i * 256];
    f32x4 zs = {0, 0, 0, 0};
    if (tid < 8) {
      const f32x4* zp = (const f32x4*)(ws + WS_ZP) + h * 128 + tid;
#pragma unroll
      for (int i = 0; i < 16; ++i) zs += zp[i * 8];
    }
    u32x4 q[4];
#pragma unroll
    for (int i = 0; i < 4; ++i) q[i] = ((const u32x4*)(ws + WS_QF))[((rg * 16 + wave * 4 + i) * 32 + h) * 64 + lane];
    f32x4 s = ((p[0] + p[1]) + (p[2] + p[3])) + ((p[4] + p[5]) + (p[6] + p[7])) + (((p[8] + p[9]) + (p[10] + p[11])) + ((p[12] + p[13]) + (p[14] + p[15])));
    {
      int fd = wave >> 1, e = (wave & 1) * 16 + (lane & 15), g = lane >> 4;
      int fe = (e >> 2) & 1, i = ((e >> 3) << 2) | (e & 3);
      *LDS3(u32x2, lds0 + ((fe * 64 + g * 16 + i) * 2 + fd) * 8) = u32x2{pack_bf16(s[0], s[1]), pack_bf16(s[2], s[3])};
      if (tid < 8) *LDS3(u32x2, lds0 + 2048 + ((tid & 3) * 2 + (tid >> 2)) * 8) = u32x2{pack_bf16(zs[0], zs[1]), pack_bf16(zs[2], zs[3])};
    }
    __syncthreads();
    bf16x8 k0 = lds_b128(lds0 + lane * 16), k1 = lds_b128(lds0 + 1024 + lane * 16), zf = lds_b128(lds0 + 2048 + (lane >> 4) * 16);
    f32x4 zero = {0, 0, 0, 0};
#pragma unroll
    for (int i = 0; i < 4; ++i) {
      bf16x8 qq = __builtin_bit_cast(bf16x8, q[i]);
      f32x4 n0 = mfma16(k0, qq, zero), n1 = mfma16(k1, qq, zero), dn = mfma16(zf, qq, zero);
      float inv = 1.f / (dn[0] + 1e-6f);
      u32x4 o = {pack_bf16(n0[0] * inv, n0[1] * inv), pack_bf16(n0[2] * inv, n0[3] * inv), pack_bf16(n1[0] * inv, n1[1] * inv), pack_bf16(n1[2] * inv, n1[3] * inv)};
      ((u32x4*)(ws + WS_OT))[((rg * 16 + wave * 4 + i) * 128 + h * 4) * 16 + lane] = o;
    }
  } else {
    int cb = b - 256, rc = cb & 3, cs = cb >> 2, mat = cs / 56, col0 = (cs % 56) * 64;
    const char* w = (const char*)(mat ? wu : wg) + ((size_t)(rc * 256 + (tid >> 3)) * FF + col0 + (tid & 7) * 8) * 2;
    u32x4 v[8];
#pragma unroll
    for (int i = 0; i < 8; ++i) v[i] = *(const u32x4*)(w + (size_t)i * 32 * FF * 2);
    float a[8];
#pragma unroll
    for (int c = 0; c < 4; ++c) {
      float lo = 0, hi = 0;
#pragma unroll
      for (int i = 0; i < 8; ++i) { lo += bf_lo(v[i][c]); hi += bf_hi(v[i][c]); }
      a[2 * c] = lo; a[2 * c + 1] = hi;
    }
#pragma unroll
    for (int c = 0; c < 8; ++c) { a[c] = sum_g(a[c]); a[c] += __shfl_xor(a[c], 8); }
    if (lane < 8) {
      *LDS3(f32x4, lds0 + (wave * 64 + lane * 8) * 4) = f32x4{a[0], a[1], a[2], a[3]};
      *LDS3(f32x4, lds0 + (wave * 64 + lane * 8 + 4) * 4) = f32x4{a[4], a[5], a[6], a[7]};
    }
    __syncthreads();
    if (tid < 64) {
      float s = (*LDS3(float, lds0 + tid * 4) + *LDS3(float, lds0 + (64 + tid) * 4)) + (*LDS3(float, lds0 + (128 + tid) * 4) + *LDS3(float, lds0 + (192 + tid) * 4));
      ((float*)(ws + WS_CS))[rc * 2 * FF + mat * FF + col0 + tid] = s;
    }
  }
  STAMPW(1, 2);
}

template <int NK> using CR = Cfg<128, 64, 64, 6, 4, 1, 2, NK, 0, 0>;
template <int MODE>
__global__ __launch_bounds__(512, 2) void kR(const void* __restrict__ w, const void* __restrict__ xres, void* __restrict__ out, char* __restrict__ ws) {
  constexpr int K = MODE ? FF : DM;
  using C = CR<K / 64>;
  STAMP(2 + 2 * MODE, 0);
  __shared__ __attribute__((aligned(256))) char sm[C::LDS];
  unsigned lds0 = (unsigned)(size_t)sm;
  int tid = threadIdx.x, lane = tid & 63, wave = __builtin_amdgcn_readfirstlane(tid >> 6);
  int wk = wave & 1, wm = wave >> 1;
  int b = blockIdx.x, xcd = b & 7, idx = b >> 3;
  int tm = (xcd >> 1) * 4 + (idx & 3), tn = (xcd & 1) * 8 + (idx >> 2);
  int g = lane >> 4, r15 = lane & 15;
  int mf = tm * 8 + wm * 2 + wk, row = mf * 16 + r15;
  u32x4 res[2];
#pragma unroll
  for (int jp = 0; jp < 2; ++jp) {
    int c8 = tn * 8 + 2 * (jp * 2 + (g & 1)) + (g >> 1);
    if (MODE == 0) res[jp] = *(const u32x4*)((const char*)xres + ((size_t)row * DM + c8 * 8) * 2);
    else res[jp] = ((const u32x4*)(ws + WS_HT))[(mf * 128 + c8) * 16 + r15];
  }
  Loader<C> L;
  loader_init<C>(L, wave, lane, ws + (MODE ? WS_ACT : WS_OT), tm * 128, K, w, w, DM, 0, [&](int lc) { return tn * 64 + lc * 8; });
  prologue<C>(L, wave, lds0);
  RdAddr<C> R; rd_init<C>(R, lds0, wm, 0, wk, lane, wk, 0, 0);
  f32x4 acc[2][4];
#pragma unroll
  for (int m = 0; m < 2; ++m)
#pragma unroll
    for (int j = 0; j < 4; ++j) acc[m][j] = f32x4{0, 0, 0, 0};
  NoHook hk;
  mainloop<C, 0xfu>(acc, L, R, wave, lds0, hk);
  STAMP(2 + 2 * MODE, 1);
#pragma unroll
  for (int j = 0; j < 4; ++j) *LDS3(f32x4, lds0 + ((wave * 4 + j) * 64 + lane) * 16) = acc[1][j];
  wait_lgkm0(); bar();
#pragma unroll
  for (int j = 0; j < 4; ++j) acc[0][j] += *LDS3(f32x4, lds0 + (((wave ^ 1) * 4 + j) * 64 + lane) * 16);
  float ss = 0, sq = 0;
#pragma unroll
  for (int jp = 0; jp < 2; ++jp) {
    f32x4 a = acc[0][2 * jp], c = acc[0][2 * jp + 1];
#pragma unroll
    for (int r = 0; r < 4; ++r) { float u = a[r], v = c[r]; swap16(u, v); a[r] = u; c[r] = v; }
    u32x4 o;
    o[0] = pack_bf16(a[0] + bf_lo(res[jp][0]), a[1] + bf_hi(res[jp][0]));
    o[1] = pack_bf16(a[2] + bf_lo(res[jp][1]), a[3] + bf_hi(res[jp][1]));
    o[2] = pack_bf16(c[0] + bf_lo(res[jp][2]), c[1] + bf_hi(res[jp][2]));
    o[3] = pack_bf16(c[2] + bf_lo(res[jp][3]), c[3] + bf_hi(res[jp][3]));
    int c8 = tn * 8 + 2 * (jp * 2 + (g & 1)) + (g >> 1);
    if (MODE == 0) {
      ((u32x4*)(ws + WS_HT))[(mf * 128 + c8) * 16 + r15] = o;
#pragma unroll
      for (int i = 0; i < 4; ++i) { ss = dot2(o[i], ONES2, ss); sq = dot2(o[i], o[i], sq); }
    } else {
      *(u32x4*)((char*)out + ((size_t)row * DM + c8 * 8) * 2) = o;
    }
  }
  if (MODE == 0) {
    ss = sum_g(ss); sq = sum_g(sq);
    if (g == 0) ((f32x2*)(ws + WS_ST))[row * 16 + tn] = f32x2{ss, sq};
  }
  STAMPW(2 + 2 * MODE, 2);
}

using CD = Cfg<256, 224, 32, 4, 4, 2, 1, 32, 0, 1>;
DEVI f32x4 gload4(const void* p) { f32x4 r; asm volatile("global_load_dwordx4 %0, %1, off" : "=v"(r) : "v"(p) : "memory"); return r; }
DEVI float gload1(const void* p) { float r; asm volatile("global_load_dword %0, %1, off" : "=v"(r) : "v"(p) : "memory"); return r; }
DEVI float geglu(float x, float u) {
  constexpr float K1 = -2.f * 0.7978845608028654f * 1.4426950408889634f, K2 = K1 * 0.044715f;
  float e = __builtin_amdgcn_exp2f(x * (K1 + K2 * x * x));
  return x * __builtin_amdgcn_rcpf(1.f + e) * u;
}
__global__ __launch_bounds__(512, 2) void kD(const void* __restrict__ wg, const void* __restrict__ wu, const void* __restrict__ wd, char* __restrict__ ws) {
  using C = CD;
  STAMP(3, 0);
  __shared__ __attribute__((aligned(256))) char sm[C::LDS + 4096];
  unsigned lds0 = (unsigned)(size_t)sm;
  constexpr unsigned O_ROW = C::LDS, O_CS = C::LDS + 2048;
  int tid = threadIdx.x, lane = tid & 63, wave = __builtin_amdgcn_readfirstlane(tid >> 6);
  int wn = wave & 1, wm = wave >> 1;
  int b = blockIdx.x, xcd = b & 7, idx = b >> 3;
  int tm = (xcd >> 2) * 4 + (idx & 3), tn = (xcd & 3) * 8 + (idx >> 2);
  {
    const char* sp = ws + WS_ST + (size_t)(tm * 256) * 128 + tid * 64;
    f32x4 s0 = gload4(sp), s1 = gload4(sp + 16), s2 = gload4(sp + 32), s3 = gload4(sp + 48);
    int ct = tid < 224 ? tid : 223;
    const float* cp = (const float*)(ws + WS_CS) + (ct < 112 ? tn * 112 + ct : FF + tn * 112 + ct - 112);
    float c0 = gload1(cp), c1 = gload1(cp + 2 * FF), c2 = gload1(cp + 4 * FF), c3 = gload1(cp + 6 * FF);
    Loader<C> L;
    loader_init<C>(L, wave, lane, ws + WS_HT, tm * 256, DM, wg, wu, FF, 0, [&](int lc) { return tn * 112 + lc * 8; });
    prologue<C>(L, wave, lds0);
    wait_stages<C, C::S>(wave);
    asm volatile("" : "+v"(s0), "+v"(s1), "+v"(s2), "+v"(s3), "+v"(c0), "+v"(c1), "+v"(c2), "+v"(c3));
    f32x4 t = (s0 + s1) + (s2 + s3);
    float s = t[0] + t[2], q = t[1] + t[3];
    s += __shfl_xor(s, 1); q += __shfl_xor(q, 1);
    float mean = s * (1.f / DM), var = fmaxf(q * (1.f / DM) - mean * mean, 0.f);
    float rstd = __builtin_amdgcn_rsqf(var + 1e-5f);
    if (!(tid & 1)) *LDS3(f32x2, lds0 + O_ROW + (tid >> 1) * 8) = f32x2{mean, rstd};
    if (tid < 224) *LDS3(float, lds0 + O_CS + tid * 4) = (c0 + c1) + (c2 + c3);
    RdAddr<C> R; rd_init<C>(R, lds0, wm, wn, 0, lane, 0, 0, 0);
    f32x4 acc[4][7];
#pragma unroll
    for (int m = 0; m < 4; ++m)
#pragma unroll
      for (int j = 0; j < 7; ++j) acc[m][j] = f32x4{0, 0, 0, 0};
    PfHook<1> hk; hk.v = lane * 64; hk.l = lds0 + C::LDS + 3072; hk.p = uni_ptr((const char*)wd + (size_t)b * 28672 + (wave < 7 ? wave : 6) * 4096);
    mainloop<C, 0x7fu>(acc, L, R, wave, lds0, hk);
    STAMP(3, 1);
    int g = lane >> 4, r15 = lane & 15, hi = g >> 1;
    f32x2 st[2];
#pragma unroll
    for (int p = 0; p < 2; ++p) st[p] = *LDS3(f32x2, lds0 + O_ROW + (wm * 64 + (p * 2 + hi) * 16 + r15) * 8);
    int smf = tm * 16 + wm * 4 + ((g & 1) * 2 + hi);
    u32x4* dst = (u32x4*)(ws + WS_ACT) + ((size_t)smf * (FF / 8) + tn * 14 + wn * 7) * 16 + r15;
#pragma unroll
    for (int j = 0; j < 7; ++j) {
      unsigned ca = lds0 + O_CS + ((wn * 7 + j) * 8 + (g & 1) * 4) * 4;
      f32x4 cg = *LDS3(f32x4, ca), cu = *LDS3(f32x4, ca + 448);
      unsigned pk[2][2];
#pragma unroll
      for (int p = 0; p < 2; ++p) {
        float a[4];
#pragma unroll
        for (int r = 0; r < 4; ++r) {
          float x = acc[2 * p][j][r], y = acc[2 * p + 1][j][r];
          swap32(x, y);
          a[r] = geglu((x - st[p][0] * cg[r]) * st[p][1], (y - st[p][0] * cu[r]) * st[p][1]);
        }
        pk[p][0] = pack_bf16(a[0], a[1]); pk[p][1] = pack_bf16(a[2], a[3]);
      }
      swap16u(pk[0][0], pk[1][0]); swap16u(pk[0][1], pk[1][1]);
      dst[j * 16] = u32x4{pk[0][0], pk[0][1], pk[1][0], pk[1][1]};
    }
  }
  STAMPW(3, 2);
}

extern "C" void kernel_launch(void* const* d_in, const int* in_sizes, int n_in, void* d_out, int out_size, void* d_ws, size_t ws_size,
                              hipStream_t stream) {
  char* ws = (char*)d_ws;
  kA<<<256, 512, 0, stream>>>(d_in[0], d_in[1], d_in[2], d_in[4], d_in[5], d_in[6], ws);
  kB<<<256 + 448, 256, 0, stream>>>(d_in[4], d_in[5], ws);
  kR<0><<<256, 512, 0, stream>>>(d_in[2], d_in[0], nullptr, ws);
  kD<<<256, 512, 0, stream>>>(d_in[4], d_in[5], d_in[6], ws);
  kR<1><<<256, 512, 0, stream>>>(d_in[6], nullptr, d_out, ws);
}
```

```cpp
#include <hip/hip_runtime.h>
#include <stdint.h>

typedef __attribute__((ext_vector_type(4))) short s16x4;
typedef __attribute__((ext_vector_type(8))) short s16x8;
typedef __attribute__((ext_vector_type(8))) __bf16 bf16x8;
typedef __attribute__((ext_vector_type(2))) __bf16 bf16x2;
typedef __attribute__((ext_vector_type(4))) float f32x4;
typedef __attribute__((ext_vector_type(2))) float f32x2;
typedef __attribute__((ext_vector_type(4))) unsigned u32x4;
typedef __attribute__((ext_vector_type(2))) unsigned u32x2;
#define LDS3(T, a) ((__attribute__((address_space(3))) T*)(size_t)(a))
#define DEVI __device__ __forceinline__

template <int N> DEVI void wait_vm() { asm volatile("s_waitcnt vmcnt(%0)" ::"n"(N) : "memory"); }
DEVI void wait_lgkm0() { asm volatile("s_waitcnt lgkmcnt(0)" ::: "memory"); }
DEVI void bar() { __builtin_amdgcn_s_barrier(); }
DEVI void glds16(unsigned voff, const char* sbase, unsigned lds) {
  asm volatile("s_mov_b32 m0, %2\n\ts_nop 0\n\tglobal_load_lds_dwordx4 %0, %1" ::"v"(voff), "s"(sbase), "s"(lds) : "memory");
}
DEVI bf16x8 lds_b128(unsigned a) { return *LDS3(bf16x8, a); }
DEVI s16x4 lds_tr(unsigned a) { return __builtin_amdgcn_ds_read_tr16_b64_v4i16(LDS3(s16x4, a)); }
DEVI unsigned pack_bf16(float lo, float hi) {
  bf16x2 v; v[0] = (__bf16)lo; v[1] = (__bf16)hi; return __builtin_bit_cast(unsigned, v);
}
DEVI float bf_lo(unsigned u) { return __uint_as_float(u << 16); }
DEVI float bf_hi(unsigned u) { return __uint_as_float(u & 0xffff0000u); }
DEVI float dot2(unsigned a, unsigned b, float c) {
  return __builtin_amdgcn_fdot2_f32_bf16(__builtin_bit_cast(bf16x2, a), __builtin_bit_cast(bf16x2, b), c, false);
}
DEVI void swap16(float& a, float& b) {
  auto r = __builtin_amdgcn_permlane16_swap(__float_as_uint(a), __float_as_uint(b), false, false);
  a = __uint_as_float(r[0]); b = __uint_as_float(r[1]);
}
DEVI void swap16u(unsigned& a, unsigned& b) {
  auto r = __builtin_amdgcn_permlane16_swap(a, b, false, false);
  a = r[0]; b = r[1];
}
DEVI void swap32(float& a, float& b) {
  auto r = __builtin_amdgcn_permlane32_swap(__float_as_uint(a), __float_as_uint(b), false, false);
  a = __uint_as_float(r[0]); b = __uint_as_float(r[1]);
}
DEVI float sum_g(float x) {
  float a = x, b = x; swap16(a, b); x = a + b;
  a = x; b = x; swap32(a, b); return a + b;
}
DEVI f32x4 mfma16(bf16x8 a, bf16x8 b, f32x4 c) { return __builtin_amdgcn_mfma_f32_16x16x32_bf16(a, b, c, 0, 0, 0); }

template <int BM_, int BN_, int BK_, int S_, int WM_, int WN_, int WK_, int NK_, int AMODE_, int BMODE_, int NPF_ = 0>
struct Cfg {
  static constexpr int NPF = NPF_;
  static constexpr int BM = BM_, BN = BN_, BK = BK_, S = S_, WM = WM_, WN = WN_, WK = WK_, NK = NK_, AMODE = AMODE_, BMODE = BMODE_;
  static constexpr int NW = WM * WN * WK;
  static constexpr int MF = BM / WM / 16, NF = BN / WN / 16, KF = BK / 32, KFW = KF / WK;
  static constexpr int A_BYTES = BM * BK * 2;
  static constexpr int B_BYTES = BN * BK * 2 + (BMODE == 1 ? 256 : 0);
  static constexpr int STAGE = A_BYTES + B_BYTES;
  static constexpr int PA = A_BYTES / 1024, PB = BN * BK * 2 / 1024, P = PA + PB;
  static constexpr int NLW = NPF ? NW - 1 : NW;
  static constexpr int NP = (P + NLW - 1) / NLW;
  static constexpr int FULLW = P - (NP - 1) * NLW;
  static constexpr int RS = (BMODE == 1 ? BN : BN * 2);
  static constexpr int LDS = S * STAGE;
  static constexpr bool SLOT = (MF == 2);
};

template <class C> struct Frags { bf16x8 a[C::KFW][C::MF]; bf16x8 b[C::KFW][C::NF]; };
template <class C> struct Loader {
  const char* sbase[C::NP]; unsigned voff[C::NP]; unsigned ldso[C::NP]; unsigned sstr[C::NP];
  const char* pfb[4]; unsigned pfv, pfl, pfs[4];
};
DEVI void glds4(unsigned voff, const char* sbase, unsigned lds) {
  asm volatile("s_mov_b32 m0, %2\n\ts_nop 0\n\tglobal_load_lds_dword %0, %1" ::"v"(voff), "s"(sbase), "s"(lds) : "memory");
}
template <class C> DEVI void issue_pf(Loader<C>& L, int i) { glds4(L.pfv, L.pfb[i], L.pfl); L.pfb[i] += L.pfs[i]; }
DEVI const char* uni_ptr(const char* p) {
  unsigned long long u = (unsigned long long)p;
  unsigned lo = __builtin_amdgcn_readfirstlane((unsigned)u), hi = __builtin_amdgcn_readfirstlane((unsigned)(u >> 32));
  return (const char*)(((unsigned long long)hi << 32) | lo);
}

template <class C, class ColMap>
DEVI void loader_init(Loader<C>& L, int wave, int lane, const void* A, int m0, int lda, const void* B0, const void* B1, int ldb, int k0,
                      ColMap colmap, int mxor = 0) {
#pragma unroll
  for (int i = 0; i < C::NP; ++i) {
    int p = wave + i * C::NLW;
    if (p >= C::P) p = C::P - 1;
    if (p < C::PA) {
      if (C::AMODE == 0) {
        int mb = p / C::KF, kb = p % C::KF;
        L.sbase[i] = (const char*)A + ((size_t)(m0 / 16 + (mb ^ mxor)) * (lda / 8) + (k0 / 8 + kb * 4)) * 256;
        L.voff[i] = lane * 16;
        L.sstr[i] = C::BK / 8 * 256;
      } else {
        int row = 8 * p + (lane >> 3), pos = lane & 7, lc = pos ^ ((row >> 1) & 7);
        L.sbase[i] = (const char*)A + ((size_t)(m0 + 8 * p) * lda + k0) * 2;
        L.voff[i] = ((lane >> 3) * lda + lc * 8) * 2;
        L.sstr[i] = C::BK * 2;
      }
      L.ldso[i] = p * 1024;
    } else {
      int pb = p - C::PA;
      if (C::BMODE == 0) {
        constexpr int CH = C::BN / 8;
        int ci = pb * 64 + lane, k = ci / CH, c = ci % CH, u = c >> 1;
        int sg = (C::RS % 256 == 0) ? ((k & 3) | (((k >> 3) & 1) << 2)) : (C::RS % 256 == 128) ? (((k >> 1) & 1) | (((k >> 3) & 1) << 1)) : ((k >> 3) & 1);
        int lc = ((u ^ sg) << 1) | (c & 1);
        L.sbase[i] = (const char*)B0 + (size_t)k0 * ldb * 2;
        L.voff[i] = (k * ldb + colmap(lc)) * 2;
        L.ldso[i] = C::A_BYTES + pb * 1024;
      } else {
        constexpr int CH = C::BN / 16, HP = C::PB / 2;
        int half = pb / HP, pp = pb % HP;
        int ci = pp * 64 + lane, k = ci / CH, c = ci % CH, lc = c ^ ((k >> 3) & 1);
        L.sbase[i] = (const char*)(half ? B1 : B0) + (size_t)k0 * ldb * 2;
        L.voff[i] = (k * ldb + colmap(lc)) * 2;
        L.ldso[i] = C::A_BYTES + half * (HP * 1024 + 128) + pp * 1024;
      }
      L.sstr[i] = C::BK * ldb * 2;
    }
    {
      unsigned long long u = (unsigned long long)L.sbase[i];
      unsigned lo = __builtin_amdgcn_readfirstlane((unsigned)u), hi = __builtin_amdgcn_readfirstlane((unsigned)(u >> 32));
      L.sbase[i] = (const char*)(((unsigned long long)hi << 32) | lo);
      L.ldso[i] = __builtin_amdgcn_readfirstlane(L.ldso[i]);
      L.sstr[i] = __builtin_amdgcn_readfirstlane(L.sstr[i]);
    }
  }
}

template <class C> DEVI void issue_piece(Loader<C>& L, int i, int wave, unsigned bufbase) {
  if (i < C::NP - 1 ? (C::NLW == C::NW || wave < C::NLW) : (C::FULLW == C::NW || wave < C::FULLW)) glds16(L.voff[i], L.sbase[i], bufbase + L.ldso[i]);
  L.sbase[i] += L.sstr[i];
}
template <class C, int NST, int X = 0> DEVI void wait_stages(int wave) {
  if (C::FULLW == C::NW || wave < C::FULLW) wait_vm<NST * C::NP + X>();
  else if (C::NLW == C::NW || wave < C::NLW) wait_vm<NST*(C::NP - 1) + X>();
}

template <class C> struct RdAddr { unsigned a[C::KFW][2]; unsigned b[C::NF]; unsigned kbo[2]; };
template <class C> DEVI void rd_init(RdAddr<C>& R, unsigned lds0, int wm, int wn, int wk, int lane, int mx, int kx, int jx) {
  int g = lane >> 4, r15 = lane & 15, q = (lane & 15) >> 2, p = lane & 3;
#pragma unroll
  for (int kb = 0; kb < C::KFW; ++kb)
#pragma unroll
    for (int m = 0; m < 2; ++m) {
      int ka = wk * C::KFW + (kb ^ kx), ma = wm * C::MF + (m ^ mx);
      if (C::AMODE == 0) R.a[kb][m] = lds0 + (ma * C::KF + ka) * 1024 + lane * 16;
      else R.a[kb][m] = lds0 + (ma * 16 + r15) * 128 + (((ka * 4 + g) ^ ((r15 >> 1) & 7)) * 16);
    }
  R.kbo[0] = kx * 32 * C::RS; R.kbo[1] = (1 ^ kx) * 32 * C::RS;
  int krow = wk * C::KFW * 32 + 8 * g + q;
#pragma unroll
  for (int j = 0; j < C::NF; ++j) {
    int U = wn * C::NF + (j ^ jx);
    if (C::BMODE == 0) {
      int sg = (C::RS % 256 == 0) ? (q | ((g & 1) << 2)) : (C::RS % 256 == 128) ? ((q >> 1) | ((g & 1) << 1)) : (g & 1);
      R.b[j] = lds0 + C::A_BYTES + krow * C::RS + ((U ^ sg) * 32) + p * 8;
    } else {
      R.b[j] = lds0 + C::A_BYTES + (p >> 1) * (C::PB / 2 * 1024 + 128) + krow * C::RS + ((U ^ (g & 1)) * 16) + (p & 1) * 8;
    }
  }
}
template <class C> DEVI void read_a(Frags<C>& f, const RdAddr<C>& R, unsigned so, int kb, int mf) {
  if (C::SLOT) f.a[kb][mf] = lds_b128(R.a[kb][mf & 1] + so);
  else f.a[kb][mf] = lds_b128(R.a[0][0] + so + (mf * C::KF + kb) * 1024);
}
template <class C> DEVI void read_b(Frags<C>& f, const RdAddr<C>& R, unsigned so, int kb, int j) {
  unsigned a = (C::AMODE == 1) ? R.b[j] + (so + R.kbo[kb & 1]) : R.b[j] + so + (kb * 32) * C::RS;
  s16x4 lo = lds_tr(a);
  s16x4 hi = lds_tr(a + 4 * C::RS);
  s16x8 v = __builtin_shufflevector(lo, hi, 0, 1, 2, 3, 4, 5, 6, 7);
  f.b[kb][j] = __builtin_bit_cast(bf16x8, v);
}
template <class C> DEVI void read_all(Frags<C>& f, const RdAddr<C>& R, unsigned so) {
#pragma unroll
  for (int kb = 0; kb < C::KFW; ++kb) {
#pragma unroll
    for (int m = 0; m < C::MF; ++m) read_a<C>(f, R, so, kb, m);
#pragma unroll
    for (int j = 0; j < C::NF; ++j) read_b<C>(f, R, so, kb, j);
  }
}

template <class C, unsigned SWAPMASK, bool ISSUE, bool READ, int NOUT, int PF, class Hook>
DEVI void iter(f32x4 (&acc)[C::MF][C::NF], const Frags<C>& cur, Frags<C>& nxt, Loader<C>& L, const RdAddr<C>& R, int wave, unsigned lds0,
               unsigned& bcur, Hook& hook) {
  wait_lgkm0();
  if (READ) { wait_stages<C, NOUT>(wave); }
  bar();
  if (PF == 9) hook.tail();
  unsigned bn = bcur + C::STAGE; if (bn == C::LDS) bn = 0;
  constexpr int NM = C::KFW * C::MF * C::NF;
  constexpr int NR = C::KFW * (C::MF + C::NF);
  constexpr int SEG = C::NP + 1;
#pragma unroll
  for (int s = 0; s < SEG; ++s) {
    if (READ) {
#pragma unroll
      for (int r = s * NR / SEG; r < (s + 1) * NR / SEG; ++r) {
        int kb = r / (C::MF + C::NF), x = r % (C::MF + C::NF);
        if (x < C::MF) read_a<C>(nxt, R, bn, kb, x); else read_b<C>(nxt, R, bn, kb, x - C::MF);
      }
    }
#pragma unroll
    for (int i = s * NM / SEG; i < (s + 1) * NM / SEG; ++i) {
      int kb = i / (C::MF * C::NF), x = i % (C::MF * C::NF), j = x / C::MF, m = x % C::MF;
      if ((SWAPMASK >> j) & 1) acc[m][j] = mfma16(cur.b[kb][j], cur.a[kb][m], acc[m][j]);
      else acc[m][j] = mfma16(cur.a[kb][m], cur.b[kb][j], acc[m][j]);
    }
    hook.seg(cur, s, SEG);
#pragma unroll
    for (int x = 0; x < 8; ++x) {
      __builtin_amdgcn_sched_group_barrier(0x8, 1, 0);
      __builtin_amdgcn_sched_group_barrier(0x100, 1, 0);
      if (Hook::VALU) __builtin_amdgcn_sched_group_barrier(0x2, Hook::VALU, 0);
    }
    __builtin_amdgcn_sched_barrier(0);
    if (PF && PF < 9 && (s == 1 || s == 3) && (PF - 1) * 2 + (s >> 1) < C::NPF) { if (wave == C::NW - 1) issue_pf<C>(L, (PF - 1) * 2 + (s >> 1)); }
    if (ISSUE && s < C::NP) { issue_piece<C>(L, s, wave, lds0 + bcur); __builtin_amdgcn_sched_barrier(0); }
  }
  bcur = bn;
}

template <class C> DEVI void prologue(Loader<C>& L, int wave, unsigned lds0) {
#pragma unroll
  for (int s = 0; s < C::S; ++s) {
#pragma unroll
    for (int i = 0; i < C::NP; ++i) issue_piece<C>(L, i, wave, lds0 + s * C::STAGE);
  }
}

template <class C, unsigned SWAPMASK, class Hook>
DEVI void mainloop(f32x4 (&acc)[C::MF][C::NF], Loader<C>& L, const RdAddr<C>& R, int wave, unsigned lds0, Hook& hook) {
  constexpr int S = C::S, NK = C::NK;
  Frags<C> f0, f1;
  wait_stages<C, S - 1>(wave);
  bar();
  read_all<C>(f0, R, 0);
  unsigned bcur = 0;
  constexpr int NMAIN = NK - S;
  constexpr int NPAIR = NMAIN / 2;
  for (int it = 0; it < NPAIR; ++it) {
    iter<C, SWAPMASK, true, true, S - 2, (C::NPF > 0 ? 1 : 0)>(acc, f0, f1, L, R, wave, lds0, bcur, hook);
    iter<C, SWAPMASK, true, true, S - 2, (C::NPF > 2 ? 2 : 0)>(acc, f1, f0, L, R, wave, lds0, bcur, hook);
  }
  constexpr int T0 = 2 * NPAIR;
#define TAILIT(T, CUR, NXT)                                                        \
  if constexpr (T0 + (T) < NK) {                                                   \
    constexpr int kt = T0 + (T);                                                   \
    constexpr bool iss = kt + S < NK, rd = kt + 1 < NK;                            \
    constexpr int last_issued = (kt + S - 1 < NK - 1) ? kt + S - 1 : NK - 1;       \
    constexpr int nout = rd ? last_issued - (kt + 1) : 0;                          \
    iter<C, SWAPMASK, iss, rd, nout, (kt == NK - 2 ? 9 : 0)>(acc, CUR, NXT, L, R, wave, lds0, bcur, hook); \
  }
  TAILIT(0, f0, f1) TAILIT(1, f1, f0) TAILIT(2, f0, f1) TAILIT(3, f1, f0) TAILIT(4, f0, f1) TAILIT(5, f1, f0) TAILIT(6, f0, f1) TAILIT(7, f1, f0)
#undef TAILIT
}
struct NoHook { static constexpr int VALU = 0; template <class F> DEVI void seg(const F&, int, int) const {} DEVI void tail() const {} };
template <int N> struct PfHook {
  static constexpr int VALU = 0;
  const char* p; unsigned v, l;
  template <class F> DEVI void seg(const F&, int, int) const {}
  DEVI void tail() const {
#pragma unroll
    for (int i = 0; i < N; ++i) glds4(v, p + i * 4096, l);
  }
};

#define PFV 1
constexpr int SEQ = 2048, DM = 1024, FF = 3584;
constexpr size_t MB = 1u << 20;
constexpr size_t WS_QF = 0;
constexpr size_t WS_OT = 4 * MB;
constexpr size_t WS_HT = 8 * MB;
constexpr size_t WS_KVP = 12 * MB;
constexpr size_t WS_ZP = 14 * MB;
constexpr size_t WS_ST = 14 * MB + 256 * 1024;
constexpr size_t WS_CS = 14 * MB + 768 * 1024;
constexpr size_t WS_ACT = 16 * MB;
constexpr size_t WS_FLG = 15 * MB;
constexpr size_t WS_XCH = 32 * MB;
#define STAMP(kid, i)
#define STAMPW(kid, i)
constexpr unsigned ONES2 = 0x3f803f80u;

DEVI float phi(float t) { return (t > 0.f ? t + 1.f : __expf(t)) + 1e-6f; }

using CA = Cfg<128, 192, 64, 3, 4, 2, 1, 16, 1, 0, 0>;
struct HookA {
  static constexpr int VALU = 0;
  f32x4 rs, rq, c[3];
  const char* p; unsigned v, l;
  DEVI void tail() const {
    glds4(v, p, l); glds4(v, p + 4096, l);
  }
  template <class F> DEVI void seg(const F& f, int s, int SEG) {
    bf16x8 ones = __builtin_bit_cast(bf16x8, u32x4{ONES2, ONES2, ONES2, ONES2});
#pragma unroll
    for (int u = s * 7 / SEG; u < (s + 1) * 7 / SEG; ++u) {
      if (u == 0) rs = mfma16(ones, f.a[0][0], rs);
      if (u == 1) rq = mfma16(f.a[0][0], f.a[0][0], rq);
      if (u == 2) c[0] = mfma16(ones, f.b[0][0], c[0]);
      if (u == 3) c[1] = mfma16(ones, f.b[0][2], c[1]);
      if (u == 4) c[2] = mfma16(ones, f.b[0][4], c[2]);
      if (u == 5) rs = mfma16(ones, f.a[1][0], rs);
      if (u == 6) rq = mfma16(f.a[1][0], f.a[1][0], rq);
    }
  }
};

__global__ __launch_bounds__(512, 2) void kA(const void* __restrict__ x, const void* __restrict__ wqkv, const void* __restrict__ wo, const void* __restrict__ wg,
                                             const void* __restrict__ wu, const void* __restrict__ wd, char* __restrict__ ws) {
  using C = CA;
  STAMP(0, 0);
  __shared__ __attribute__((aligned(256))) char sm[C::LDS + 256];
  unsigned lds0 = (unsigned)(size_t)sm;
  int tid = threadIdx.x, lane = tid & 63, wave = __builtin_amdgcn_readfirstlane(tid >> 6);
  int wn = wave & 1, wm = wave >> 1;
  int b = blockIdx.x, xcd = b & 7, idx = b >> 3;
  int tm = (xcd >> 1) * 4 + (idx & 3), tn = (xcd & 1) * 8 + (idx >> 2);
  Loader<C> L;
  loader_init<C>(L, wave, lane, x, tm * 128, DM, wqkv, wqkv, 3 * DM, 0, [&](int lc) {
    int c = lc * 8, w = c / 96, r = c % 96;
    return (r >> 5) * DM + (2 * tn + w) * 32 + (r & 31);
  });
  prologue<C>(L, wave, lds0);
  int mx = wn, kx = wm & 1, jx = wm >> 1;
  RdAddr<C> R; rd_init<C>(R, lds0, wm, wn, 0, lane, mx, kx, jx);
  f32x4 acc[2][6];
#pragma unroll
  for (int m = 0; m < 2; ++m)
#pragma unroll
    for (int j = 0; j < 6; ++j) acc[m][j] = f32x4{0, 0, 0, 0};
  HookA hk; hk.v = lane * 64; hk.l = lds0 + C::LDS;
  {
    int s0 = wave * 2;
    const char* pp = s0 < 7 ? (const char*)wg + (size_t)b * 28672 + (s0 < 6 ? s0 : 5) * 4096 : s0 < 14 ? (const char*)wu + (size_t)b * 28672 + (s0 - 7) * 4096 : (const char*)wo + (size_t)b * 8192;
    hk.p = uni_ptr(pp);
    if (wave == 3) glds4(hk.v, uni_ptr((const char*)wu + (size_t)b * 28672), hk.l);
  }
  hk.rs = hk.rq = hk.c[0] = hk.c[1] = hk.c[2] = f32x4{0, 0, 0, 0};
  mainloop<C, 0x3u>(acc, L, R, wave, lds0, hk);
  STAMP(0, 1);

  int g = lane >> 4, r15 = lane & 15;
  constexpr unsigned O_ROW = 0, O_CS = 1024, O_Z = 4096, O_RED = 8192;
  {
    int rr = r15 & 3;
    float s = hk.rs[0], q = rr == 0 ? hk.rq[0] : rr == 1 ? hk.rq[1] : rr == 2 ? hk.rq[2] : hk.rq[3];
    float mean = s * (1.f / DM), var = fmaxf(q * (1.f / DM) - mean * mean, 0.f);
    float rstd = __builtin_amdgcn_rsqf(var + 1e-5f);
    if (g == (r15 >> 2)) *LDS3(f32x2, lds0 + O_ROW + (wm * 32 + wn * 16 + r15) * 8) = f32x2{mean, rstd};
    float cc = g == 1 ? hk.c[0][0] : g == 2 ? hk.c[1][0] : hk.c[2][0];
    if (g) *LDS3(float, lds0 + O_CS + (kx * 192 + wn * 96 + (((g - 1) * 2) ^ jx) * 16 + r15) * 4) = cc;
  }
  wait_lgkm0(); bar();
  int head = 2 * tn + wn;
#pragma unroll
  for (int m = 0; m < 2; ++m) {
    int ma = m ^ mx;
    f32x2 st = *LDS3(f32x2, lds0 + O_ROW + (wm * 32 + ma * 16 + r15) * 8);
    unsigned pk[2][2];
#pragma unroll
    for (int j = 0; j < 2; ++j) {
      unsigned ca = lds0 + O_CS + (wn * 96 + (j ^ jx) * 16 + 4 * g) * 4;
      f32x4 cs = *LDS3(f32x4, ca) + *LDS3(f32x4, ca + 768);
      float v[4];
#pragma unroll
      for (int r = 0; r < 4; ++r) v[r] = phi((acc[m][j][r] - st[0] * cs[r]) * st[1]);
      pk[j][0] = pack_bf16(v[0], v[1]); pk[j][1] = pack_bf16(v[2], v[3]);
    }
    u32x4 o = jx ? u32x4{pk[1][0], pk[1][1], pk[0][0], pk[0][1]} : u32x4{pk[0][0], pk[0][1], pk[1][0], pk[1][1]};
    ((u32x4*)(ws + WS_QF))[((tm * 8 + wm * 2 + ma) * 32 + head) * 64 + lane] = o;
  }
  {
    f32x4 sa[2][2];
#pragma unroll
    for (int m = 0; m < 2; ++m) {
      unsigned ra = lds0 + O_ROW + (wm * 32 + (m ^ mx) * 16 + 4 * g) * 8;
      sa[m][0] = *LDS3(f32x4, ra); sa[m][1] = *LDS3(f32x4, ra + 16);
    }
    bf16x8 fr[4];
#pragma unroll
    for (int j = 2; j < 6; ++j) {
      unsigned ca = lds0 + O_CS + (wn * 96 + (j ^ jx) * 16 + r15) * 4;
      float cs = *LDS3(float, ca) + *LDS3(float, ca + 768);
      u32x4 u;
#pragma unroll
      for (int m = 0; m < 2; ++m) {
        float v[4];
#pragma unroll
        for (int r = 0; r < 4; ++r) {
          float t = (acc[m][j][r] - sa[m][r >> 1][(r & 1) * 2] * cs) * sa[m][r >> 1][(r & 1) * 2 + 1];
          v[r] = j < 4 ? phi(t) : t;
        }
        u[m * 2] = pack_bf16(v[0], v[1]); u[m * 2 + 1] = pack_bf16(v[2], v[3]);
      }
      fr[j - 2] = __builtin_bit_cast(bf16x8, u);
    }
    bf16x8 ones = __builtin_bit_cast(bf16x8, u32x4{ONES2, ONES2, ONES2, ONES2});
    f32x4 zero = {0, 0, 0, 0};
#pragma unroll
    for (int fd = 0; fd < 2; ++fd) {
#pragma unroll
      for (int fe = 0; fe < 2; ++fe) {
        f32x4 kv = mfma16(fr[fd], fr[2 + fe], zero);
        *LDS3(f32x4, lds0 + O_RED + ((wave * 4 + ((fd ^ jx) * 2 + (fe ^ jx))) * 64 + lane) * 16) = kv;
      }
      f32x4 z = mfma16(fr[fd], ones, zero);
      if (r15 == 0) *LDS3(f32x4, lds0 + O_Z + ((wave * 2 + (fd ^ jx)) * 4 + g) * 16) = z;
    }
  }
  wait_lgkm0(); bar();
  {
    int w2 = tid >> 8, i = tid & 255;
    f32x4 s = *LDS3(f32x4, lds0 + O_RED + (w2 * 256 + i) * 16);
#pragma unroll
    for (int k = 1; k < 4; ++k) s += *LDS3(f32x4, lds0 + O_RED + ((k * 2 + w2) * 256 + i) * 16);
    ((f32x4*)(ws + WS_KVP))[((2 * tn + w2) * 16 + tm) * 256 + i] = s;
    if (tid < 16) {
      int w3 = tid >> 3, c = tid & 7;
      f32x4 z = *LDS3(f32x4, lds0 + O_Z + (w3 * 8 + c) * 16);
#pragma unroll
      for (int k = 1; k < 4; ++k) z += *LDS3(f32x4, lds0 + O_Z + ((k * 2 + w3) * 8 + c) * 16);
      ((f32x4*)(ws + WS_ZP))[((2 * tn + w3) * 16 + tm) * 8 + c] = z;
    }
  }
  STAMPW(0, 2);
}

__global__ __launch_bounds__(256) void kB(const void* __restrict__ wg, const void* __restrict__ wu, char* __restrict__ ws) {
  __shared__ __attribute__((aligned(16))) char sm[4096];
  unsigned lds0 = (unsigned)(size_t)sm;
  int tid = threadIdx.x, lane = tid & 63, wave = tid >> 6, b = blockIdx.x;
  STAMP(1, 0); STAMP(1, 1);
  if (b < 256) {
    int h = (b & 7) * 4 + ((b >> 3) & 3), rg = b >> 5;
    if (tid < 8) *(unsigned*)(ws + WS_FLG + (b * 8 + tid) * 128) = 0u;
    const f32x4* kvp = (const f32x4*)(ws + WS_KVP) + h * 4096 + tid;
    f32x4 p[16];
#pragma unroll
    for (int i = 0; i < 16; ++i) p[i] = kvp[i * 256];
    f32x4 zs = {0, 0, 0, 0};
    if (tid < 8) {
      const f32x4* zp = (const f32x4*)(ws + WS_ZP) + h * 128 + tid;
#pragma unroll
      for (int i = 0; i < 16; ++i) zs += zp[i * 8];
    }
    u32x4 q[4];
#pragma unroll
    for (int i = 0; i < 4; ++i) q[i] = ((const u32x4*)(ws + WS_QF))[((rg * 16 + wave * 4 + i) * 32 + h) * 64 + lane];
    f32x4 s = ((p[0] + p[1]) + (p[2] + p[3])) + ((p[4] + p[5]) + (p[6] + p[7])) + (((p[8] + p[9]) + (p[10] + p[11])) + ((p[12] + p[13]) + (p[14] + p[15])));
    {
      int fd = wave >> 1, e = (wave & 1) * 16 + (lane & 15), g = lane >> 4;
      int fe = (e >> 2) & 1, i = ((e >> 3) << 2) | (e & 3);
      *LDS3(u32x2, lds0 + ((fe * 64 + g * 16 + i) * 2 + fd) * 8) = u32x2{pack_bf16(s[0], s[1]), pack_bf16(s[2], s[3])};
      if (tid < 8) *LDS3(u32x2, lds0 + 2048 + ((tid & 3) * 2 + (tid >> 2)) * 8) = u32x2{pack_bf16(zs[0], zs[1]), pack_bf16(zs[2], zs[3])};
    }
    __syncthreads();
    bf16x8 k0 = lds_b128(lds0 + lane * 16), k1 = lds_b128(lds0 + 1024 + lane * 16), zf = lds_b128(lds0 + 2048 + (lane >> 4) * 16);
    f32x4 zero = {0, 0, 0, 0};
#pragma unroll
    for (int i = 0; i < 4; ++i) {
      bf16x8 qq = __builtin_bit_cast(bf16x8, q[i]);
      f32x4 n0 = mfma16(k0, qq, zero), n1 = mfma16(k1, qq, zero), dn = mfma16(zf, qq, zero);
      float inv = 1.f / (dn[0] + 1e-6f);
      u32x4 o = {pack_bf16(n0[0] * inv, n0[1] * inv), pack_bf16(n0[2] * inv, n0[3] * inv), pack_bf16(n1[0] * inv, n1[1] * inv), pack_bf16(n1[2] * inv, n1[3] * inv)};
      ((u32x4*)(ws + WS_OT))[((rg * 16 + wave * 4 + i) * 128 + h * 4) * 16 + lane] = o;
    }
  } else {
    int cb = b - 256, rc = cb & 3, cs = cb >> 2, mat = cs / 56, col0 = (cs % 56) * 64;
    const char* w = (const char*)(mat ? wu : wg) + ((size_t)(rc * 256 + (tid >> 3)) * FF + col0 + (tid & 7) * 8) * 2;
    u32x4 v[8];
#pragma unroll
    for (int i = 0; i < 8; ++i) v[i] = *(const u32x4*)(w + (size_t)i * 32 * FF * 2);
    float a[8];
#pragma unroll
    for (int c = 0; c < 4; ++c) {
      float lo = 0, hi = 0;
#pragma unroll
      for (int i = 0; i < 8; ++i) { lo += bf_lo(v[i][c]); hi += bf_hi(v[i][c]); }
      a[2 * c] = lo; a[2 * c + 1] = hi;
    }
#pragma unroll
    for (int c = 0; c < 8; ++c) { a[c] = sum_g(a[c]); a[c] += __shfl_xor(a[c], 8); }
    if (lane < 8) {
      *LDS3(f32x4, lds0 + (wave * 64 + lane * 8) * 4) = f32x4{a[0], a[1], a[2], a[3]};
      *LDS3(f32x4, lds0 + (wave * 64 + lane * 8 + 4) * 4) = f32x4{a[4], a[5], a[6], a[7]};
    }
    __syncthreads();
    if (tid < 64) {
      float s = (*LDS3(float, lds0 + tid * 4) + *LDS3(float, lds0 + (64 + tid) * 4)) + (*LDS3(float, lds0 + (128 + tid) * 4) + *LDS3(float, lds0 + (192 + tid) * 4));
      ((float*)(ws + WS_CS))[rc * 2 * FF + mat * FF + col0 + tid] = s;
    }
  }
  STAMPW(1, 2);
}

template <int NK> using CR = Cfg<128, 64, 64, 6, 4, 1, 2, NK, 0, 0>;
template <int MODE>
__global__ __launch_bounds__(512, 2) void kR(const void* __restrict__ w, const void* __restrict__ xres, void* __restrict__ out, char* __restrict__ ws) {
  constexpr int K = MODE ? FF : DM;
  using C = CR<K / 64>;
  STAMP(2 + 2 * MODE, 0);
  __shared__ __attribute__((aligned(256))) char sm[C::LDS];
  unsigned lds0 = (unsigned)(size_t)sm;
  int tid = threadIdx.x, lane = tid & 63, wave = __builtin_amdgcn_readfirstlane(tid >> 6);
  int wk = wave & 1, wm = wave >> 1;
  int b = blockIdx.x, xcd = b & 7, idx = b >> 3;
  int tm = (xcd >> 1) * 4 + (idx & 3), tn = (xcd & 1) * 8 + (idx >> 2);
  int g = lane >> 4, r15 = lane & 15;
  int mf = tm * 8 + wm * 2 + wk, row = mf * 16 + r15;
  u32x4 res[2];
#pragma unroll
  for (int jp = 0; jp < 2; ++jp) {
    int c8 = tn * 8 + 2 * (jp * 2 + (g & 1)) + (g >> 1);
    if (MODE == 0) res[jp] = *(const u32x4*)((const char*)xres + ((size_t)row * DM + c8 * 8) * 2);
    else res[jp] = ((const u32x4*)(ws + WS_HT))[(mf * 128 + c8) * 16 + r15];
  }
  Loader<C> L;
  loader_init<C>(L, wave, lane, ws + (MODE ? WS_ACT : WS_OT), tm * 128, K, w, w, DM, 0, [&](int lc) { return tn * 64 + lc * 8; });
  prologue<C>(L, wave, lds0);
  RdAddr<C> R; rd_init<C>(R, lds0, wm, 0, wk, lane, wk, 0, 0);
  f32x4 acc[2][4];
#pragma unroll
  for (int m = 0; m < 2; ++m)
#pragma unroll
    for (int j = 0; j < 4; ++j) acc[m][j] = f32x4{0, 0, 0, 0};
  NoHook hk;
  mainloop<C, 0xfu>(acc, L, R, wave, lds0, hk);
  STAMP(2 + 2 * MODE, 1);
#pragma unroll
  for (int j = 0; j < 4; ++j) *LDS3(f32x4, lds0 + ((wave * 4 + j) * 64 + lane) * 16) = acc[1][j];
  wait_lgkm0(); bar();
#pragma unroll
  for (int j = 0; j < 4; ++j) acc[0][j] += *LDS3(f32x4, lds0 + (((wave ^ 1) * 4 + j) * 64 + lane) * 16);
  float ss = 0, sq = 0;
#pragma unroll
  for (int jp = 0; jp < 2; ++jp) {
    f32x4 a = acc[0][2 * jp], c = acc[0][2 * jp + 1];
#pragma unroll
    for (int r = 0; r < 4; ++r) { float u = a[r], v = c[r]; swap16(u, v); a[r] = u; c[r] = v; }
    u32x4 o;
    o[0] = pack_bf16(a[0] + bf_lo(res[jp][0]), a[1] + bf_hi(res[jp][0]));
    o[1] = pack_bf16(a[2] + bf_lo(res[jp][1]), a[3] + bf_hi(res[jp][1]));
    o[2] = pack_bf16(c[0] + bf_lo(res[jp][2]), c[1] + bf_hi(res[jp][2]));
    o[3] = pack_bf16(c[2] + bf_lo(res[jp][3]), c[3] + bf_hi(res[jp][3]));
    int c8 = tn * 8 + 2 * (jp * 2 + (g & 1)) + (g >> 1);
    if (MODE == 0) {
      ((u32x4*)(ws + WS_HT))[(mf * 128 + c8) * 16 + r15] = o;
#pragma unroll
      for (int i = 0; i < 4; ++i) { ss = dot2(o[i], ONES2, ss); sq = dot2(o[i], o[i], sq); }
    } else {
      *(u32x4*)((char*)out + ((size_t)row * DM + c8 * 8) * 2) = o;
    }
  }
  if (MODE == 0) {
    ss = sum_g(ss); sq = sum_g(sq);
    if (g == 0) ((f32x2*)(ws + WS_ST))[row * 16 + tn] = f32x2{ss, sq};
  }
  STAMPW(2 + 2 * MODE, 2);
}

using CD = Cfg<256, 224, 32, 4, 4, 2, 1, 32, 0, 1>;
DEVI f32x4 gload4(const void* p) { f32x4 r; asm volatile("global_load_dwordx4 %0, %1, off" : "=v"(r) : "v"(p) : "memory"); return r; }
DEVI float gload1(const void* p) { float r; asm volatile("global_load_dword %0, %1, off" : "=v"(r) : "v"(p) : "memory"); return r; }
DEVI float geglu(float x, float u) {
  constexpr float K1 = -2.f * 0.7978845608028654f * 1.4426950408889634f, K2 = K1 * 0.044715f;
  float e = __builtin_amdgcn_exp2f(x * (K1 + K2 * x * x));
  return x * __builtin_amdgcn_rcpf(1.f + e) * u;
}
__global__ __launch_bounds__(512, 2) void kD(const void* __restrict__ wg, const void* __restrict__ wu, const void* __restrict__ wd, char* __restrict__ ws) {
  using C = CD;
  STAMP(3, 0);
  __shared__ __attribute__((aligned(256))) char sm[C::LDS + 4096];
  unsigned lds0 = (unsigned)(size_t)sm;
  constexpr unsigned O_ROW = C::LDS, O_CS = C::LDS + 2048;
  int tid = threadIdx.x, lane = tid & 63, wave = __builtin_amdgcn_readfirstlane(tid >> 6);
  int wn = wave & 1, wm = wave >> 1;
  int b = blockIdx.x, xcd = b & 7, idx = b >> 3;
  int tm = (xcd >> 2) * 4 + (idx & 3), tn = (xcd & 3) * 8 + (idx >> 2);
  {
    const char* sp = ws + WS_ST + (size_t)(tm * 256) * 128 + tid * 64;
    f32x4 s0 = gload4(sp), s1 = gload4(sp + 16), s2 = gload4(sp + 32), s3 = gload4(sp + 48);
    int ct = tid < 224 ? tid : 223;
    const float* cp = (const float*)(ws + WS_CS) + (ct < 112 ? tn * 112 + ct : FF + tn * 112 + ct - 112);
    float c0 = gload1(cp), c1 = gload1(cp + 2 * FF), c2 = gload1(cp + 4 * FF), c3 = gload1(cp + 6 * FF);
    Loader<C> L;
    loader_init<C>(L, wave, lane, ws + WS_HT, tm * 256, DM, wg, wu, FF, 0, [&](int lc) { return tn * 112 + lc * 8; });
    prologue<C>(L, wave, lds0);
    wait_stages<C, C::S>(wave);
    asm volatile("" : "+v"(s0), "+v"(s1), "+v"(s2), "+v"(s3), "+v"(c0), "+v"(c1), "+v"(c2), "+v"(c3));
    f32x4 t = (s0 + s1) + (s2 + s3);
    float s = t[0] + t[2], q = t[1] + t[3];
    s += __shfl_xor(s, 1); q += __shfl_xor(q, 1);
    float mean = s * (1.f / DM), var = fmaxf(q * (1.f / DM) - mean * mean, 0.f);
    float rstd = __builtin_amdgcn_rsqf(var + 1e-5f);
    if (!(tid & 1)) *LDS3(f32x2, lds0 + O_ROW + (tid >> 1) * 8) = f32x2{mean, rstd};
    if (tid < 224) *LDS3(float, lds0 + O_CS + tid * 4) = (c0 + c1) + (c2 + c3);
    RdAddr<C> R; rd_init<C>(R, lds0, wm, wn, 0, lane, 0, 0, 0);
    f32x4 acc[4][7];
#pragma unroll
    for (int m = 0; m < 4; ++m)
#pragma unroll
      for (int j = 0; j < 7; ++j) acc[m][j] = f32x4{0, 0, 0, 0};
    PfHook<1> hk; hk.v = lane * 64; hk.l = lds0 + C::LDS + 3072; hk.p = uni_ptr((const char*)wd + (size_t)b * 28672 + (wave < 7 ? wave : 6) * 4096);
    mainloop<C, 0x7fu>(acc, L, R, wave, lds0, hk);
    STAMP(3, 1);
    int g = lane >> 4, r15 = lane & 15, hi = g >> 1;
    f32x2 st[2];
#pragma unroll
    for (int p = 0; p < 2; ++p) st[p] = *LDS3(f32x2, lds0 + O_ROW + (wm * 64 + (p * 2 + hi) * 16 + r15) * 8);
    int smf = tm * 16 + wm * 4 + ((g & 1) * 2 + hi);
    u32x4* dst = (u32x4*)(ws + WS_ACT) + ((size_t)smf * (FF / 8) + tn * 14 + wn * 7) * 16 + r15;
#pragma unroll
    for (int j = 0; j < 7; ++j) {
      unsigned ca = lds0 + O_CS + ((wn * 7 + j) * 8 + (g & 1) * 4) * 4;
      f32x4 cg = *LDS3(f32x4, ca), cu = *LDS3(f32x4, ca + 448);
      unsigned pk[2][2];
#pragma unroll
      for (int p = 0; p < 2; ++p) {
        float a[4];
#pragma unroll
        for (int r = 0; r < 4; ++r) {
          float x = acc[2 * p][j][r], y = acc[2 * p + 1][j][r];
          swap32(x, y);
          a[r] = geglu((x - st[p][0] * cg[r]) * st[p][1], (y - st[p][0] * cu[r]) * st[p][1]);
        }
        pk[p][0] = pack_bf16(a[0], a[1]); pk[p][1] = pack_bf16(a[2], a[3]);
      }
      swap16u(pk[0][0], pk[1][0]); swap16u(pk[0][1], pk[1][1]);
      dst[j * 16] = u32x4{pk[0][0], pk[0][1], pk[1][0], pk[1][1]};
    }
  }
  STAMPW(3, 2);
}

using CE = Cfg<128, 128, 64, 4, 2, 2, 2, FF / 128, 0, 0>;
typedef __attribute__((address_space(1))) unsigned gu32;
__global__ __launch_bounds__(512, 2) void kE(const void* __restrict__ w, void* __restrict__ out, char* __restrict__ ws) {
  using C = CE;
  STAMP(4, 0);
  __shared__ __attribute__((aligned(256))) char sm[C::LDS];
  unsigned lds0 = (unsigned)(size_t)sm;
  int tid = threadIdx.x, lane = tid & 63, wave = __builtin_amdgcn_readfirstlane(tid >> 6);
  int wk = wave & 1, wn = (wave >> 1) & 1, wm = wave >> 2;
  int b = blockIdx.x, xcd = b & 7, idx = b >> 3;
  int kh = xcd & 1, tm = (xcd >> 1) * 4 + (idx & 3), tn = idx >> 2;
  int g = lane >> 4, r15 = lane & 15;
  int c8 = tn * 16 + wn * 8 + 2 * (2 * wk + (g & 1)) + (g >> 1);
  int mf0 = tm * 8 + wm * 4 + 2 * kh;
  u32x4 res[2];
#pragma unroll
  for (int m = 0; m < 2; ++m) res[m] = ((const u32x4*)(ws + WS_HT))[((mf0 + m) * 128 + c8) * 16 + r15];
  Loader<C> L;
  loader_init<C>(L, wave, lane, ws + WS_ACT, tm * 128, FF, w, w, DM, kh * (FF / 2), [&](int lc) { return tn * 128 + lc * 8; }, 2 * kh);
  prologue<C>(L, wave, lds0);
  RdAddr<C> R; rd_init<C>(R, lds0, wm, wn, wk, lane, 0, 0, 2 * wk);
  f32x4 acc[4][4];
#pragma unroll
  for (int m = 0; m < 4; ++m)
#pragma unroll
    for (int j = 0; j < 4; ++j) acc[m][j] = f32x4{0, 0, 0, 0};
  NoHook hk;
  mainloop<C, 0xfu>(acc, L, R, wave, lds0, hk);
  STAMP(4, 1);
#pragma unroll
  for (int m = 3; m >= 0; --m)
#pragma unroll
    for (int j = 0; j < 2; ++j) *LDS3(f32x4, lds0 + ((wave * 8 + m * 2 + j) * 64 + lane) * 16) = acc[m][2 + j];
  wait_lgkm0(); bar();
  const __amdgpu_buffer_rsrc_t rs = __builtin_amdgcn_make_buffer_rsrc(ws + WS_XCH, 0, 256 * 8 * 4096, 0x00020000);
#pragma unroll
  for (int m = 3; m >= 0; --m)
#pragma unroll
    for (int j = 0; j < 2; ++j) {
      acc[m][j] += *LDS3(f32x4, lds0 + (((wave ^ 1) * 8 + m * 2 + j) * 64 + lane) * 16);
      if (m >= 2) __builtin_amdgcn_raw_buffer_store_b128(__builtin_bit_cast(u32x4, acc[m][j]), rs, (((b * 8 + wave) * 4 + (m - 2) * 2 + j) * 64 + lane) * 16, 0, 16);
    }
  asm volatile("s_waitcnt vmcnt(0)" ::: "memory");
  if (lane == 0) __hip_atomic_store((gu32*)(ws + WS_FLG + (b * 8 + wave) * 128), 1u, __ATOMIC_RELAXED, __HIP_MEMORY_SCOPE_AGENT);
  {
    gu32* pf = (gu32*)(ws + WS_FLG + ((b ^ 1) * 8 + wave) * 128);
    for (int spin = 0; spin < (1 << 22); ++spin) {
      if (__builtin_amdgcn_readfirstlane(__hip_atomic_load(pf, __ATOMIC_RELAXED, __HIP_MEMORY_SCOPE_AGENT)) == 1u) break;
      __builtin_amdgcn_s_sleep(1);
    }
    asm volatile("" ::: "memory");
  }
  u32x4 x[4];
#pragma unroll
  for (int i = 0; i < 4; ++i) x[i] = __builtin_amdgcn_raw_buffer_load_b128(rs, ((((b ^ 1) * 8 + wave) * 4 + i) * 64 + lane) * 16, 0, 16);
#pragma unroll
  for (int m = 0; m < 2; ++m) {
    f32x4 a = acc[m][0] + __builtin_bit_cast(f32x4, x[2 * m]), c = acc[m][1] + __builtin_bit_cast(f32x4, x[2 * m + 1]);
#pragma unroll
    for (int r = 0; r < 4; ++r) { float u = a[r], v = c[r]; swap16(u, v); a[r] = u; c[r] = v; }
    u32x4 o;
    o[0] = pack_bf16(a[0] + bf_lo(res[m][0]), a[1] + bf_hi(res[m][0]));
    o[1] = pack_bf16(a[2] + bf_lo(res[m][1]), a[3] + bf_hi(res[m][1]));
    o[2] = pack_bf16(c[0] + bf_lo(res[m][2]), c[1] + bf_hi(res[m][2]));
    o[3] = pack_bf16(c[2] + bf_lo(res[m][3]), c[3] + bf_hi(res[m][3]));
    *(u32x4*)((char*)out + ((size_t)((mf0 + m) * 16 + r15) * DM + c8 * 8) * 2) = o;
  }
  STAMPW(4, 2);
}

extern "C" void kernel_launch(void* const* d_in, const int* in_sizes, int n_in, void* d_out, int out_size, void* d_ws, size_t ws_size,
                              hipStream_t stream) {
  char* ws = (char*)d_ws;
  kA<<<256, 512, 0, stream>>>(d_in[0], d_in[1], d_in[2], d_in[4], d_in[5], d_in[6], ws);
  kB<<<256 + 448, 256, 0, stream>>>(d_in[4], d_in[5], ws);
  kR<0><<<256, 512, 0, stream>>>(d_in[2], d_in[0], nullptr, ws);
  kD<<<256, 512, 0, stream>>>(d_in[4], d_in[5], d_in[6], ws);
  kE<<<256, 512, 0, stream>>>(d_in[6], d_out, ws);
}
```

```cpp
#include <hip/hip_runtime.h>
#include <stdint.h>
#define STAGE_D 0
#define STAG 2

typedef __attribute__((ext_vector_type(4))) short s16x4;
typedef __attribute__((ext_vector_type(8))) short s16x8;
typedef __attribute__((ext_vector_type(8))) __bf16 bf16x8;
typedef __attribute__((ext_vector_type(2))) __bf16 bf16x2;
typedef __attribute__((ext_vector_type(4))) float f32x4;
typedef __attribute__((ext_vector_type(2))) float f32x2;
typedef __attribute__((ext_vector_type(4))) unsigned u32x4;
typedef __attribute__((ext_vector_type(2))) unsigned u32x2;
#define LDS3(T, a) ((__attribute__((address_space(3))) T*)(size_t)(a))
#define DEVI __device__ __forceinline__

template <int N> DEVI void wait_vm() { asm volatile("s_waitcnt vmcnt(%0)" ::"n"(N) : "memory"); }
DEVI void wait_lgkm0() { asm volatile("s_waitcnt lgkmcnt(0)" ::: "memory"); }
DEVI void bar() { __builtin_amdgcn_s_barrier(); }
DEVI void glds16(unsigned voff, const char* sbase, unsigned lds) {
  asm volatile("s_mov_b32 m0, %2\n\ts_nop 0\n\tglobal_load_lds_dwordx4 %0, %1" ::"v"(voff), "s"(sbase), "s"(lds) : "memory");
}
DEVI bf16x8 lds_b128(unsigned a) { return *LDS3(bf16x8, a); }
DEVI s16x4 lds_tr(unsigned a) { return __builtin_amdgcn_ds_read_tr16_b64_v4i16(LDS3(s16x4, a)); }
DEVI unsigned pack_bf16(float lo, float hi) {
  bf16x2 v; v[0] = (__bf16)lo; v[1] = (__bf16)hi; return __builtin_bit_cast(unsigned, v);
}
DEVI float bf_lo(unsigned u) { return __uint_as_float(u << 16); }
DEVI float bf_hi(unsigned u) { return __uint_as_float(u & 0xffff0000u); }
DEVI float dot2(unsigned a, unsigned b, float c) {
  return __builtin_amdgcn_fdot2_f32_bf16(__builtin_bit_cast(bf16x2, a), __builtin_bit_cast(bf16x2, b), c, false);
}
DEVI void swap16(float& a, float& b) {
  auto r = __builtin_amdgcn_permlane16_swap(__float_as_uint(a), __float_as_uint(b), false, false);
  a = __uint_as_float(r[0]); b = __uint_as_float(r[1]);
}
DEVI void swap16u(unsigned& a, unsigned& b) {
  auto r = __builtin_amdgcn_permlane16_swap(a, b, false, false);
  a = r[0]; b = r[1];
}
DEVI void swap32(float& a, float& b) {
  auto r = __builtin_amdgcn_permlane32_swap(__float_as_uint(a), __float_as_uint(b), false, false);
  a = __uint_as_float(r[0]); b = __uint_as_float(r[1]);
}
DEVI float sum_g(float x) {
  float a = x, b = x; swap16(a, b); x = a + b;
  a = x; b = x; swap32(a, b); return a + b;
}
DEVI f32x4 mfma16(bf16x8 a, bf16x8 b, f32x4 c) { return __builtin_amdgcn_mfma_f32_16x16x32_bf16(a, b, c, 0, 0, 0); }

template <int BM_, int BN_, int BK_, int S_, int WM_, int WN_, int WK_, int NK_, int AMODE_, int BMODE_, int NPF_ = 0>
struct Cfg {
  static constexpr int NPF = NPF_;
  static constexpr int BM = BM_, BN = BN_, BK = BK_, S = S_, WM = WM_, WN = WN_, WK = WK_, NK = NK_, AMODE = AMODE_, BMODE = BMODE_;
  static constexpr int NW = WM * WN * WK;
  static constexpr int MF = BM / WM / 16, NF = BN / WN / 16, KF = BK / 32, KFW = KF / WK;
  static constexpr int A_BYTES = BM * BK * 2;
  static constexpr int B_BYTES = BN * BK * 2 + (BMODE == 1 ? 256 : 0);
  static constexpr int STAGE = A_BYTES + B_BYTES;
  static constexpr int PA = A_BYTES / 1024, PB = BN * BK * 2 / 1024, P = PA + PB;
  static constexpr int NLW = NPF ? NW - 1 : NW;
  static constexpr int NP = (P + NLW - 1) / NLW;
  static constexpr int FULLW = P - (NP - 1) * NLW;
  static constexpr int RS = (BMODE == 1 ? BN : BN * 2);
  static constexpr int LDS = S * STAGE;
  static constexpr bool SLOT = (MF == 2);
};

template <class C> struct Frags { bf16x8 a[C::KFW][C::MF]; bf16x8 b[C::KFW][C::NF]; };
template <class C> struct Loader {
  const char* sbase[C::NP]; unsigned voff[C::NP]; unsigned ldso[C::NP]; unsigned sstr[C::NP];
  const char* pfb[4]; unsigned pfv, pfl, pfs[4];
};
DEVI void glds4(unsigned voff, const char* sbase, unsigned lds) {
  asm volatile("s_mov_b32 m0, %2\n\ts_nop 0\n\tglobal_load_lds_dword %0, %1" ::"v"(voff), "s"(sbase), "s"(lds) : "memory");
}
template <class C> DEVI void issue_pf(Loader<C>& L, int i) { glds4(L.pfv, L.pfb[i], L.pfl); L.pfb[i] += L.pfs[i]; }
DEVI const char* uni_ptr(const char* p) {
  unsigned long long u = (unsigned long long)p;
  unsigned lo = __builtin_amdgcn_readfirstlane((unsigned)u), hi = __builtin_amdgcn_readfirstlane((unsigned)(u >> 32));
  return (const char*)(((unsigned long long)hi << 32) | lo);
}

template <class C> DEVI void issue_piece(Loader<C>& L, int i, int wave, unsigned bufbase) {
  if (i < C::NP - 1 ? (C::NLW == C::NW || wave < C::NLW) : (C::FULLW == C::NW || wave < C::FULLW)) glds16(L.voff[i], L.sbase[i], bufbase + L.ldso[i]);
  L.sbase[i] += L.sstr[i];
}
template <class C, class ColMap>
DEVI void loader_init(Loader<C>& L, unsigned lds0, int wave, int lane, const void* A, int m0, int lda, const void* B0, const void* B1, int ldb, int k0,
                      ColMap colmap, int mxor = 0) {
#pragma unroll
  for (int i = 0; i < C::NP; ++i) {
    int p = wave + i * C::NLW;
    if (p >= C::P) p = C::P - 1;
    if (p < C::PA) {
      if (C::AMODE == 0) {
        int mb = p / C::KF, kb = p % C::KF;
        L.sbase[i] = (const char*)A + ((size_t)(m0 / 16 + (mb ^ mxor)) * (lda / 8) + (k0 / 8 + kb * 4)) * 256;
        L.voff[i] = lane * 16;
        L.sstr[i] = C::BK / 8 * 256;
      } else {
        int row = 8 * p + (lane >> 3), pos = lane & 7, lc = pos ^ ((row >> 1) & 7);
        L.sbase[i] = (const char*)A + ((size_t)(m0 + 8 * p) * lda + k0) * 2;
        L.voff[i] = ((lane >> 3) * lda + lc * 8) * 2;
        L.sstr[i] = C::BK * 2;
      }
      L.ldso[i] = p * 1024;
    } else {
      int pb = p - C::PA;
      if (C::BMODE == 0) {
        constexpr int CH = C::BN / 8;
        int ci = pb * 64 + lane, k = ci / CH, c = ci % CH, u = c >> 1;
        int sg = (C::RS % 256 == 0) ? ((k & 3) | (((k >> 3) & 1) << 2)) : (C::RS % 256 == 128) ? (((k >> 1) & 1) | (((k >> 3) & 1) << 1)) : ((k >> 3) & 1);
        int lc = ((u ^ sg) << 1) | (c & 1);
        L.sbase[i] = (const char*)B0 + (size_t)k0 * ldb * 2;
        L.voff[i] = (k * ldb + colmap(lc)) * 2;
        L.ldso[i] = C::A_BYTES + pb * 1024;
      } else {
        constexpr int CH = C::BN / 16, HP = C::PB / 2;
        int half = pb / HP, pp = pb % HP;
        int ci = pp * 64 + lane, k = ci / CH, c = ci % CH, lc = c ^ ((k >> 3) & 1);
        L.sbase[i] = (const char*)(half ? B1 : B0) + (size_t)k0 * ldb * 2;
        L.voff[i] = (k * ldb + colmap(lc)) * 2;
        L.ldso[i] = C::A_BYTES + half * (HP * 1024 + 128) + pp * 1024;
      }
      L.sstr[i] = C::BK * ldb * 2;
    }
    {
      unsigned long long u = (unsigned long long)L.sbase[i];
      unsigned lo = __builtin_amdgcn_readfirstlane((unsigned)u), hi = __builtin_amdgcn_readfirstlane((unsigned)(u >> 32));
      L.sbase[i] = (const char*)(((unsigned long long)hi << 32) | lo);
      L.ldso[i] = __builtin_amdgcn_readfirstlane(L.ldso[i]);
      L.sstr[i] = __builtin_amdgcn_readfirstlane(L.sstr[i]);
    }
    issue_piece<C>(L, i, wave, lds0);
  }
}

template <class C, int NST, int X = 0> DEVI void wait_stages(int wave) {
  if (C::FULLW == C::NW || wave < C::FULLW) wait_vm<NST * C::NP + X>();
  else if (C::NLW == C::NW || wave < C::NLW) wait_vm<NST*(C::NP - 1) + X>();
}

template <class C> struct RdAddr { unsigned a[C::KFW][2]; unsigned b[C::NF]; unsigned kbo[2]; };
template <class C> DEVI void rd_init(RdAddr<C>& R, unsigned lds0, int wm, int wn, int wk, int lane, int mx, int kx, int jx) {
  int g = lane >> 4, r15 = lane & 15, q = (lane & 15) >> 2, p = lane & 3;
#pragma unroll
  for (int kb = 0; kb < C::KFW; ++kb)
#pragma unroll
    for (int m = 0; m < 2; ++m) {
      int ka = wk * C::KFW + (kb ^ kx), ma = wm * C::MF + (m ^ mx);
      if (C::AMODE == 0) R.a[kb][m] = lds0 + (ma * C::KF + ka) * 1024 + lane * 16;
      else R.a[kb][m] = lds0 + (ma * 16 + r15) * 128 + (((ka * 4 + g) ^ ((r15 >> 1) & 7)) * 16);
    }
  R.kbo[0] = kx * 32 * C::RS; R.kbo[1] = (1 ^ kx) * 32 * C::RS;
  int krow = wk * C::KFW * 32 + 8 * g + q;
#pragma unroll
  for (int j = 0; j < C::NF; ++j) {
    int U = wn * C::NF + (j ^ jx);
    if (C::BMODE == 0) {
      int sg = (C::RS % 256 == 0) ? (q | ((g & 1) << 2)) : (C::RS % 256 == 128) ? ((q >> 1) | ((g & 1) << 1)) : (g & 1);
      R.b[j] = lds0 + C::A_BYTES + krow * C::RS + ((U ^ sg) * 32) + p * 8;
    } else {
      R.b[j] = lds0 + C::A_BYTES + (p >> 1) * (C::PB / 2 * 1024 + 128) + krow * C::RS + ((U ^ (g & 1)) * 16) + (p & 1) * 8;
    }
  }
}
template <class C> DEVI void read_a(Frags<C>& f, const RdAddr<C>& R, unsigned so, int kb, int mf) {
  if (C::SLOT) f.a[kb][mf] = lds_b128(R.a[kb][mf & 1] + so);
  else f.a[kb][mf] = lds_b128(R.a[0][0] + so + (mf * C::KF + kb) * 1024);
}
template <class C> DEVI void read_b(Frags<C>& f, const RdAddr<C>& R, unsigned so, int kb, int j) {
  unsigned a = (C::AMODE == 1) ? R.b[j] + (so + R.kbo[kb & 1]) : R.b[j] + so + (kb * 32) * C::RS;
  s16x4 lo = lds_tr(a);
  s16x4 hi = lds_tr(a + 4 * C::RS);
  s16x8 v = __builtin_shufflevector(lo, hi, 0, 1, 2, 3, 4, 5, 6, 7);
  f.b[kb][j] = __builtin_bit_cast(bf16x8, v);
}
template <class C> DEVI void read_all(Frags<C>& f, const RdAddr<C>& R, unsigned so) {
#pragma unroll
  for (int kb = 0; kb < C::KFW; ++kb) {
#pragma unroll
    for (int m = 0; m < C::MF; ++m) read_a<C>(f, R, so, kb, m);
#pragma unroll
    for (int j = 0; j < C::NF; ++j) read_b<C>(f, R, so, kb, j);
  }
}

template <class C, unsigned SWAPMASK, bool ISSUE, bool READ, int NOUT, int PF, class Hook>
DEVI void iter(f32x4 (&acc)[C::MF][C::NF], const Frags<C>& cur, Frags<C>& nxt, Loader<C>& L, const RdAddr<C>& R, int wave, unsigned lds0,
               unsigned& bcur, Hook& hook, int kt) {
  wait_lgkm0();
  if (READ) { wait_stages<C, NOUT, (PF == 10 ? Hook::NT : 0)>(wave); }
  bar();
  if (PF == 9) hook.tail();
  unsigned bn = bcur + C::STAGE; if (bn == C::LDS) bn = 0;
  constexpr int NM = C::KFW * C::MF * C::NF;
  constexpr int NR = C::KFW * (C::MF + C::NF);
  constexpr int SEG = C::NP + 1;
#pragma unroll
  for (int s = 0; s < SEG; ++s) {
    if (READ) {
#pragma unroll
      for (int r = s * NR / SEG; r < (s + 1) * NR / SEG; ++r) {
        int kb = r / (C::MF + C::NF), x = r % (C::MF + C::NF);
        if (x < C::MF) read_a<C>(nxt, R, bn, kb, x); else read_b<C>(nxt, R, bn, kb, x - C::MF);
      }
    }
#pragma unroll
    for (int i = s * NM / SEG; i < (s + 1) * NM / SEG; ++i) {
      int kb = i / (C::MF * C::NF), x = i % (C::MF * C::NF), j = x / C::MF, m = x % C::MF;
      if ((SWAPMASK >> j) & 1) acc[m][j] = mfma16(cur.b[kb][j], cur.a[kb][m], acc[m][j]);
      else acc[m][j] = mfma16(cur.a[kb][m], cur.b[kb][j], acc[m][j]);
    }
    hook.seg(cur, s, SEG);
#pragma unroll
    for (int x = 0; x < 8; ++x) {
      __builtin_amdgcn_sched_group_barrier(0x8, 1, 0);
      __builtin_amdgcn_sched_group_barrier(0x100, 1, 0);
      if (Hook::VALU) __builtin_amdgcn_sched_group_barrier(0x2, Hook::VALU, 0);
    }
    __builtin_amdgcn_sched_barrier(0);
    if (PF && PF < 9 && (s == 1 || s == 3) && (PF - 1) * 2 + (s >> 1) < C::NPF) { if (wave == C::NW - 1) issue_pf<C>(L, (PF - 1) * 2 + (s >> 1)); }
    if (ISSUE && s < C::NP) { issue_piece<C>(L, s, wave, lds0 + bcur); __builtin_amdgcn_sched_barrier(0); }
    if (s == SEG - 1) hook.mid(kt);
  }
  bcur = bn;
}

template <class C> DEVI void prologue(Loader<C>& L, int wave, unsigned lds0) {
#pragma unroll
  for (int s = 1; s < C::S; ++s) {
#pragma unroll
    for (int i = 0; i < C::NP; ++i) issue_piece<C>(L, i, wave, lds0 + s * C::STAGE);
  }
}

template <class C, unsigned SWAPMASK, class Hook>
DEVI void mainloop(f32x4 (&acc)[C::MF][C::NF], Loader<C>& L, const RdAddr<C>& R, int wave, unsigned lds0, Hook& hook) {
  constexpr int S = C::S, NK = C::NK;
  Frags<C> f0, f1;
  wait_stages<C, S - 1>(wave);
  bar();
  read_all<C>(f0, R, 0);
  unsigned bcur = 0;
  constexpr int NMAIN = NK - S;
  constexpr int NPAIR = NMAIN / 2;
  for (int it = 0; it < NPAIR; ++it) {
    iter<C, SWAPMASK, true, true, S - 2, (C::NPF > 0 ? 1 : 0)>(acc, f0, f1, L, R, wave, lds0, bcur, hook, 2 * it);
    iter<C, SWAPMASK, true, true, S - 2, (C::NPF > 2 ? 2 : 0)>(acc, f1, f0, L, R, wave, lds0, bcur, hook, 2 * it + 1);
  }
  constexpr int T0 = 2 * NPAIR;
#define TAILIT(T, CUR, NXT)                                                        \
  if constexpr (T0 + (T) < NK) {                                                   \
    constexpr int kt = T0 + (T);                                                   \
    constexpr bool iss = kt + S < NK, rd = kt + 1 < NK;                            \
    constexpr int last_issued = (kt + S - 1 < NK - 1) ? kt + S - 1 : NK - 1;       \
    constexpr int nout = rd ? last_issued - (kt + 1) : 0;                          \
    iter<C, SWAPMASK, iss, rd, nout, (kt == NK - S ? 9 : kt > NK - S ? 10 : 0)>(acc, CUR, NXT, L, R, wave, lds0, bcur, hook, kt); \
  }
  TAILIT(0, f0, f1) TAILIT(1, f1, f0) TAILIT(2, f0, f1) TAILIT(3, f1, f0) TAILIT(4, f0, f1) TAILIT(5, f1, f0) TAILIT(6, f0, f1) TAILIT(7, f1, f0)
#undef TAILIT
}
struct NoHook { static constexpr int VALU = 0, NT = 0; template <class F> DEVI void seg(const F&, int, int) const {} DEVI void tail() const {} DEVI void mid(int) const {} };
template <int N> struct PfHook {
  static constexpr int VALU = 0, NT = (STAGE_D ? 0 : N);
  const char* p; unsigned v, l; int k1;
  template <class F> DEVI void seg(const F&, int, int) const {}
  DEVI void tail() const {
#pragma unroll
    for (int i = 0; i < N; ++i) glds4(v, p + i * 4096, l);
  }
  DEVI void mid(int kt) const {
  }
};

#define TNX 0
#define LINPF 2
#define DST 16
#define CSA 1
#define STAGD 4
#define STAG0 3
constexpr int SEQ = 2048, DM = 1024, FF = 3584;
constexpr size_t MB = 1u << 20;
constexpr size_t WS_QF = 0;
constexpr size_t WS_OT = 4 * MB;
constexpr size_t WS_HT = 8 * MB;
constexpr size_t WS_KVP = 12 * MB;
constexpr size_t WS_ZP = 14 * MB;
constexpr size_t WS_ST = 14 * MB + 256 * 1024;
constexpr size_t WS_CS = 14 * MB + 768 * 1024;
constexpr size_t WS_ACT = 16 * MB;
constexpr size_t WS_FLG = 15 * MB + 512 * 1024;
constexpr size_t WS_XCH = 32 * MB;
#define DIAG3 0
#define STAMP3(kid, pos)
#define STAMP(kid, i)
#define STAMPW(kid, i)
constexpr unsigned ONES2 = 0x3f803f80u;
#define WTM 15
template <bool WT> DEVI void st16(void* base, unsigned bytes, unsigned off, u32x4 v) {
  if (WT) __builtin_amdgcn_raw_buffer_store_b128(v, __builtin_amdgcn_make_buffer_rsrc(base, 0, bytes, 0x00020000), off, 0, 16);
  else *(u32x4*)((char*)base + off) = v;
}

DEVI f32x4 gload4(const void* p) { f32x4 r; asm volatile("global_load_dwordx4 %0, %1, off" : "=v"(r) : "v"(p) : "memory"); return r; }
DEVI float gload1(const void* p) { float r; asm volatile("global_load_dword %0, %1, off" : "=v"(r) : "v"(p) : "memory"); return r; }
DEVI float phi(float t) { return (t > 0.f ? t + 1.f : __expf(t)) + 1e-6f; }

using CA = Cfg<128, 192, 64, 3, 4, 2, 1, 16, 1, 0, 0>;
struct HookA {
  static constexpr int VALU = 0, NT = (STAG == 0 ? 2 : STAG == 2 ? 1 : 0);
  f32x4 rs, rq, c[3];
  const char* p; unsigned v, v2, l; int k1;
  DEVI void tail() const {
    glds4(v2, p, l);
  }
  DEVI void mid(int kt) const {
    if (kt == k1) glds4(v, p, l);
    if (STAG == 1 && kt == k1 + STAGD) glds4(v2, p, l);
  }
  template <class F> DEVI void seg(const F& f, int s, int SEG) {
    bf16x8 ones = __builtin_bit_cast(bf16x8, u32x4{ONES2, ONES2, ONES2, ONES2});
#pragma unroll
    for (int u = s * 7 / SEG; u < (s + 1) * 7 / SEG; ++u) {
      if (u == 0) rs = mfma16(ones, f.a[0][0], rs);
      if (u == 1) rq = mfma16(f.a[0][0], f.a[0][0], rq);
      if (u == 2) c[0] = mfma16(ones, f.b[0][0], c[0]);
      if (u == 3) c[1] = mfma16(ones, f.b[0][2], c[1]);
      if (u == 4) c[2] = mfma16(ones, f.b[0][4], c[2]);
      if (u == 5) rs = mfma16(ones, f.a[1][0], rs);
      if (u == 6) rq = mfma16(f.a[1][0], f.a[1][0], rq);
    }
  }
};

__global__ __launch_bounds__(512, 2) void kA(const void* __restrict__ x, const void* __restrict__ wqkv, const void* __restrict__ wo, const void* __restrict__ wg,
                                             const void* __restrict__ wu, const void* __restrict__ wd, char* __restrict__ ws) {
  using C = CA;
  STAMP(0, 0);
  __shared__ __attribute__((aligned(256))) char sm[C::LDS + 256];
  unsigned lds0 = (unsigned)(size_t)sm;
  int tid = threadIdx.x, lane = tid & 63, wave = __builtin_amdgcn_readfirstlane(tid >> 6);
  int wn = wave & 1, wm = wave >> 1;
  int b = blockIdx.x, xcd = b & 7, idx = b >> 3;
  int tm = (xcd >> 1) * 4 + (idx & 3), tn = (xcd & 1) * 8 + ((idx >> 2) ^ TNX);
  Loader<C> L;
  loader_init<C>(L, lds0, wave, lane, x, tm * 128, DM, wqkv, wqkv, 3 * DM, 0, [&](int lc) {
    int c = lc * 8, w = c / 96, r = c % 96;
    return (r >> 5) * DM + (2 * tn + w) * 32 + (r & 31);
  });
  STAMP3(0, 1);
  prologue<C>(L, wave, lds0);
  STAMP3(0, 2);
  if (wave < 6) glds4(lane * 64, uni_ptr((const char*)wqkv + (size_t)b * 24576 + wave * 4096), lds0 + C::LDS);
  int mx = wn, kx = wm & 1, jx = wm >> 1;
  RdAddr<C> R; rd_init<C>(R, lds0, wm, wn, 0, lane, mx, kx, jx);
  f32x4 acc[2][6];
#pragma unroll
  for (int m = 0; m < 2; ++m)
#pragma unroll
    for (int j = 0; j < 6; ++j) acc[m][j] = f32x4{0, 0, 0, 0};
  HookA hk; hk.l = lds0 + C::LDS;
  const char* wp = uni_ptr((const char*)(b >> 7 ? wu : wg) + (size_t)((b >> 3) & 15) * 64 * FF * 2 + (b & 7) * 896);
  {
    int i1 = wave * 128 + lane, i2 = i1 + 64;
    hk.p = wave < 7 ? wp : uni_ptr((const char*)wo + (size_t)b * 8192);
    hk.v = wave < 7 ? (i1 / 14) * (FF * 2) + (i1 % 14) * 64 : lane * 64;
    hk.v2 = wave < 7 ? (i2 / 14) * (FF * 2) + (i2 % 14) * 64 : lane * 64 + 4096;
    hk.k1 = STAG0 + wave;
  }
  hk.rs = hk.rq = hk.c[0] = hk.c[1] = hk.c[2] = f32x4{0, 0, 0, 0};
  mainloop<C, 0x3u>(acc, L, R, wave, lds0, hk);
  STAMP(0, 1);
  u32x4 wv[8];
  if (CSA) {
    int l56 = lane < 56 ? lane : 55;
#pragma unroll
    for (int i = 0; i < 8; ++i) wv[i] = *(const __attribute__((address_space(1))) u32x4*)(size_t)(wp + (wave * 8 + i) * (FF * 2) + l56 * 16);
  }
  int g = lane >> 4, r15 = lane & 15;
  constexpr unsigned O_ROW = 0, O_CS = 1024, O_Z = 4096, O_RED = 8192, O_WP = 40960;
  {
    int rr = r15 & 3;
    float s = hk.rs[0], q = rr == 0 ? hk.rq[0] : rr == 1 ? hk.rq[1] : rr == 2 ? hk.rq[2] : hk.rq[3];
    float mean = s * (1.f / DM), var = fmaxf(q * (1.f / DM) - mean * mean, 0.f);
    float rstd = __builtin_amdgcn_rsqf(var + 1e-5f);
    if (g == (r15 >> 2)) *LDS3(f32x2, lds0 + O_ROW + (wm * 32 + wn * 16 + r15) * 8) = f32x2{mean, rstd};
    float cc = g == 1 ? hk.c[0][0] : g == 2 ? hk.c[1][0] : hk.c[2][0];
    if (g) *LDS3(float, lds0 + O_CS + (kx * 192 + wn * 96 + (((g - 1) * 2) ^ jx) * 16 + r15) * 4) = cc;
  }
  wait_lgkm0(); bar();
  int head = 2 * tn + wn;
#pragma unroll
  for (int m = 0; m < 2; ++m) {
    int ma = m ^ mx;
    f32x2 st = *LDS3(f32x2, lds0 + O_ROW + (wm * 32 + ma * 16 + r15) * 8);
    unsigned pk[2][2];
#pragma unroll
    for (int j = 0; j < 2; ++j) {
      unsigned ca = lds0 + O_CS + (wn * 96 + (j ^ jx) * 16 + 4 * g) * 4;
      f32x4 cs = *LDS3(f32x4, ca) + *LDS3(f32x4, ca + 768);
      float v[4];
#pragma unroll
      for (int r = 0; r < 4; ++r) v[r] = phi((acc[m][j][r] - st[0] * cs[r]) * st[1]);
      pk[j][0] = pack_bf16(v[0], v[1]); pk[j][1] = pack_bf16(v[2], v[3]);
    }
    u32x4 o = jx ? u32x4{pk[1][0], pk[1][1], pk[0][0], pk[0][1]} : u32x4{pk[0][0], pk[0][1], pk[1][0], pk[1][1]};
    st16<WTM & 1>(ws + WS_QF, 4 * MB, (((tm * 8 + wm * 2 + ma) * 32 + head) * 64 + lane) * 16, o);
  }
  {
    f32x4 sa[2][2];
#pragma unroll
    for (int m = 0; m < 2; ++m) {
      unsigned ra = lds0 + O_ROW + (wm * 32 + (m ^ mx) * 16 + 4 * g) * 8;
      sa[m][0] = *LDS3(f32x4, ra); sa[m][1] = *LDS3(f32x4, ra + 16);
    }
    bf16x8 fr[4];
#pragma unroll
    for (int j = 2; j < 6; ++j) {
      unsigned ca = lds0 + O_CS + (wn * 96 + (j ^ jx) * 16 + r15) * 4;
      float cs = *LDS3(float, ca) + *LDS3(float, ca + 768);
      u32x4 u;
#pragma unroll
      for (int m = 0; m < 2; ++m) {
        float v[4];
#pragma unroll
        for (int r = 0; r < 4; ++r) {
          float t = (acc[m][j][r] - sa[m][r >> 1][(r & 1) * 2] * cs) * sa[m][r >> 1][(r & 1) * 2 + 1];
          v[r] = j < 4 ? phi(t) : t;
        }
        u[m * 2] = pack_bf16(v[0], v[1]); u[m * 2 + 1] = pack_bf16(v[2], v[3]);
      }
      fr[j - 2] = __builtin_bit_cast(bf16x8, u);
    }
    bf16x8 ones = __builtin_bit_cast(bf16x8, u32x4{ONES2, ONES2, ONES2, ONES2});
    f32x4 zero = {0, 0, 0, 0};
#pragma unroll
    for (int fd = 0; fd < 2; ++fd) {
#pragma unroll
      for (int fe = 0; fe < 2; ++fe) {
        f32x4 kv = mfma16(fr[fd], fr[2 + fe], zero);
        *LDS3(f32x4, lds0 + O_RED + ((wave * 4 + ((fd ^ jx) * 2 + (fe ^ jx))) * 64 + lane) * 16) = kv;
      }
      f32x4 z = mfma16(fr[fd], ones, zero);
      if (r15 == 0) *LDS3(f32x4, lds0 + O_Z + ((wave * 2 + (fd ^ jx)) * 4 + g) * 16) = z;
    }
  }
  if (CSA) {
    f32x4 a[2];
#pragma unroll
    for (int c = 0; c < 4; ++c) {
      float lo = 0, hi = 0;
#pragma unroll
      for (int i = 0; i < 8; ++i) { lo += bf_lo(wv[i][c]); hi += bf_hi(wv[i][c]); }
      a[c >> 1][(c & 1) * 2] = lo; a[c >> 1][(c & 1) * 2 + 1] = hi;
    }
    *LDS3(f32x4, lds0 + O_WP + wave * 2048 + lane * 32) = a[0];
    *LDS3(f32x4, lds0 + O_WP + wave * 2048 + lane * 32 + 16) = a[1];
  }
  wait_lgkm0(); bar();
  if (CSA && tid >= 384 && tid < 496) {
    int t = tid - 384;
    f32x4 s = *LDS3(f32x4, lds0 + O_WP + t * 16);
#pragma unroll
    for (int k = 1; k < 8; ++k) s += *LDS3(f32x4, lds0 + O_WP + k * 2048 + t * 16);
    st16<WTM & 1>(ws + WS_CS, 16 * 2 * FF * 4, (((b >> 3) & 15) * 2 * FF + (b >> 7) * FF + (b & 7) * 448 + t * 4) * 4, __builtin_bit_cast(u32x4, s));
  }
  {
    int w2 = tid >> 8, i = tid & 255;
    f32x4 s = *LDS3(f32x4, lds0 + O_RED + (w2 * 256 + i) * 16);
#pragma unroll
    for (int k = 1; k < 4; ++k) s += *LDS3(f32x4, lds0 + O_RED + ((k * 2 + w2) * 256 + i) * 16);
    st16<WTM & 1>(ws + WS_KVP, 2 * MB, (((2 * tn + w2) * 16 + tm) * 256 + i) * 16, __builtin_bit_cast(u32x4, s));
    if (tid < 16) {
      int w3 = tid >> 3, c = tid & 7;
      f32x4 z = *LDS3(f32x4, lds0 + O_Z + (w3 * 8 + c) * 16);
#pragma unroll
      for (int k = 1; k < 4; ++k) z += *LDS3(f32x4, lds0 + O_Z + ((k * 2 + w3) * 8 + c) * 16);
      ((f32x4*)(ws + WS_ZP))[((2 * tn + w3) * 16 + tm) * 8 + c] = z;
    }
  }
  STAMPW(0, 2);
}

__global__ __launch_bounds__(256) void kB(const void* __restrict__ wg, const void* __restrict__ wu, char* __restrict__ ws) {
  __shared__ __attribute__((aligned(16))) char sm[8192];
  unsigned lds0 = (unsigned)(size_t)sm;
  int tid = threadIdx.x, lane = tid & 63, wave = tid >> 6, b = blockIdx.x;
  STAMP(1, 0); STAMP(1, 1);
  if (CSA || b < 256) {
    int h = (b & 7) * 4 + ((b >> 3) & 3), rg = b >> 5;
    if (tid < 8) *(unsigned*)(ws + WS_FLG + (b * 8 + tid) * 128) = 0u;
    const f32x4* kvp = (const f32x4*)(ws + WS_KVP) + h * 4096 + tid;
    const f32x4* zp = (const f32x4*)(ws + WS_ZP) + h * 128 + lane;
    f32x4 z0 = gload4(zp), z1 = gload4(zp + 64);
    f32x4 p[16];
#pragma unroll
    for (int i = 0; i < 16; ++i) p[i] = gload4(kvp + i * 256);
    u32x4 q[4];
#pragma unroll
    for (int i = 0; i < 4; ++i) q[i] = __builtin_bit_cast(u32x4, gload4((const u32x4*)(ws + WS_QF) + ((rg * 16 + wave * 4 + i) * 32 + h) * 64 + lane));
    asm volatile("s_waitcnt vmcnt(4)" : "+v"(z0), "+v"(z1), "+v"(p[0]), "+v"(p[1]), "+v"(p[2]), "+v"(p[3]), "+v"(p[4]), "+v"(p[5]), "+v"(p[6]), "+v"(p[7])::"memory");
    asm volatile("" : "+v"(p[8]), "+v"(p[9]), "+v"(p[10]), "+v"(p[11]), "+v"(p[12]), "+v"(p[13]), "+v"(p[14]), "+v"(p[15])::"memory");
    f32x4 zs = z0 + z1;
#pragma unroll
    for (int c = 0; c < 4; ++c) { zs[c] = sum_g(zs[c]); zs[c] += __shfl_xor(zs[c], 8); }
    f32x4 s = ((p[0] + p[1]) + (p[2] + p[3])) + ((p[4] + p[5]) + (p[6] + p[7])) + (((p[8] + p[9]) + (p[10] + p[11])) + ((p[12] + p[13]) + (p[14] + p[15])));
    {
      int fd = wave >> 1, e = (wave & 1) * 16 + (lane & 15), g = lane >> 4;
      int fe = (e >> 2) & 1, i = ((e >> 3) << 2) | (e & 3);
      *LDS3(u32x2, lds0 + ((fe * 64 + g * 16 + i) * 2 + fd) * 8) = u32x2{pack_bf16(s[0], s[1]), pack_bf16(s[2], s[3])};
      if (tid < 8) *LDS3(u32x2, lds0 + 2048 + ((tid & 3) * 2 + (tid >> 2)) * 8) = u32x2{pack_bf16(zs[0], zs[1]), pack_bf16(zs[2], zs[3])};
    }
    wait_lgkm0(); bar();
    asm volatile("s_waitcnt vmcnt(0)" : "+v"(q[0]), "+v"(q[1]), "+v"(q[2]), "+v"(q[3])::"memory");
    bf16x8 k0 = lds_b128(lds0 + lane * 16), k1 = lds_b128(lds0 + 1024 + lane * 16), zf = lds_b128(lds0 + 2048 + (lane >> 4) * 16);
    f32x4 zero = {0, 0, 0, 0};
#pragma unroll
    for (int i = 0; i < 4; ++i) {
      bf16x8 qq = __builtin_bit_cast(bf16x8, q[i]);
      f32x4 n0 = mfma16(k0, qq, zero), n1 = mfma16(k1, qq, zero), dn = mfma16(zf, qq, zero);
      float inv = 1.f / (dn[0] + 1e-6f);
      u32x4 o = {pack_bf16(n0[0] * inv, n0[1] * inv), pack_bf16(n0[2] * inv, n0[3] * inv), pack_bf16(n1[0] * inv, n1[1] * inv), pack_bf16(n1[2] * inv, n1[3] * inv)};
      st16<(WTM & 2) != 0>(ws + WS_OT, 4 * MB, (((rg * 16 + wave * 4 + i) * 128 + h * 4) * 16 + lane) * 16, o);
    }
  } else {
    int cb = b - 256, l56 = lane < 56 ? lane : 55;
    const char* wp = (const char*)(cb >> 7 ? wu : wg) + (size_t)(((cb >> 3) & 15) * 64 + wave * 16) * FF * 2 + (cb & 7) * 896 + l56 * 16;
    u32x4 v[16];
#pragma unroll
    for (int i = 0; i < 16; ++i) v[i] = *(const u32x4*)(wp + i * FF * 2);
    f32x4 a[2];
#pragma unroll
    for (int c = 0; c < 4; ++c) {
      float lo = 0, hi = 0;
#pragma unroll
      for (int i = 0; i < 16; ++i) { lo += bf_lo(v[i][c]); hi += bf_hi(v[i][c]); }
      a[c >> 1][(c & 1) * 2] = lo; a[c >> 1][(c & 1) * 2 + 1] = hi;
    }
    *LDS3(f32x4, lds0 + wave * 2048 + lane * 32) = a[0];
    *LDS3(f32x4, lds0 + wave * 2048 + lane * 32 + 16) = a[1];
    __syncthreads();
    if (tid < 112) {
      f32x4 s = (*LDS3(f32x4, lds0 + tid * 16) + *LDS3(f32x4, lds0 + 2048 + tid * 16)) + (*LDS3(f32x4, lds0 + 4096 + tid * 16) + *LDS3(f32x4, lds0 + 6144 + tid * 16));
      st16<(WTM & 2) != 0>(ws + WS_CS, 16 * 2 * FF * 4, (((cb >> 3) & 15) * 2 * FF + (cb >> 7) * FF + (cb & 7) * 448 + tid * 4) * 4, __builtin_bit_cast(u32x4, s));
    }
  }
  STAMPW(1, 2);
}

template <int NK> using CR = Cfg<128, 64, 64, 6, 4, 1, 2, NK, 0, 0>;
template <int MODE>
__global__ __launch_bounds__(512, 2) void kR(const void* __restrict__ w, const void* __restrict__ xres, void* __restrict__ out, char* __restrict__ ws) {
  constexpr int K = MODE ? FF : DM;
  using C = CR<K / 64>;
  STAMP(2 + 2 * MODE, 0);
  __shared__ __attribute__((aligned(256))) char sm[C::LDS];
  unsigned lds0 = (unsigned)(size_t)sm;
  int tid = threadIdx.x, lane = tid & 63, wave = __builtin_amdgcn_readfirstlane(tid >> 6);
  int wk = wave & 1, wm = wave >> 1;
  int b = blockIdx.x, xcd = b & 7, idx = b >> 3;
  int tm = (xcd >> 1) * 4 + (idx & 3), tn = (xcd & 1) * 8 + (idx >> 2);
  int g = lane >> 4, r15 = lane & 15;
  int mf = tm * 8 + wm * 2 + wk, row = mf * 16 + r15;
  u32x4 res[2];
#pragma unroll
  for (int jp = 0; jp < 2; ++jp) {
    int c8 = tn * 8 + 2 * (jp * 2 + (g & 1)) + (g >> 1);
    if (MODE == 0) res[jp] = *(const u32x4*)((const char*)xres + ((size_t)row * DM + c8 * 8) * 2);
    else res[jp] = ((const u32x4*)(ws + WS_HT))[(mf * 128 + c8) * 16 + r15];
  }
  Loader<C> L;
  loader_init<C>(L, lds0, wave, lane, ws + (MODE ? WS_ACT : WS_OT), tm * 128, K, w, w, DM, 0, [&](int lc) { return tn * 64 + lc * 8; });
  STAMP3(2, 1);
  prologue<C>(L, wave, lds0);
  STAMP3(2, 2);
  RdAddr<C> R; rd_init<C>(R, lds0, wm, 0, wk, lane, wk, 0, 0);
  f32x4 acc[2][4];
#pragma unroll
  for (int m = 0; m < 2; ++m)
#pragma unroll
    for (int j = 0; j < 4; ++j) acc[m][j] = f32x4{0, 0, 0, 0};
  NoHook hk;
  mainloop<C, 0xfu>(acc, L, R, wave, lds0, hk);
  STAMP(2 + 2 * MODE, 1);
#pragma unroll
  for (int j = 0; j < 4; ++j) *LDS3(f32x4, lds0 + ((wave * 4 + j) * 64 + lane) * 16) = acc[1][j];
  wait_lgkm0(); bar();
#pragma unroll
  for (int j = 0; j < 4; ++j) acc[0][j] += *LDS3(f32x4, lds0 + (((wave ^ 1) * 4 + j) * 64 + lane) * 16);
  float ss = 0, sq = 0;
#pragma unroll
  for (int jp = 0; jp < 2; ++jp) {
    f32x4 a = acc[0][2 * jp], c = acc[0][2 * jp + 1];
#pragma unroll
    for (int r = 0; r < 4; ++r) { float u = a[r], v = c[r]; swap16(u, v); a[r] = u; c[r] = v; }
    u32x4 o;
    o[0] = pack_bf16(a[0] + bf_lo(res[jp][0]), a[1] + bf_hi(res[jp][0]));
    o[1] = pack_bf16(a[2] + bf_lo(res[jp][1]), a[3] + bf_hi(res[jp][1]));
    o[2] = pack_bf16(c[0] + bf_lo(res[jp][2]), c[1] + bf_hi(res[jp][2]));
    o[3] = pack_bf16(c[2] + bf_lo(res[jp][3]), c[3] + bf_hi(res[jp][3]));
    int c8 = tn * 8 + 2 * (jp * 2 + (g & 1)) + (g >> 1);
    if (MODE == 0) {
      st16<(WTM & 4) != 0>(ws + WS_HT, 4 * MB, ((mf * 128 + c8) * 16 + r15) * 16, o);
#pragma unroll
      for (int i = 0; i < 4; ++i) { ss = dot2(o[i], ONES2, ss); sq = dot2(o[i], o[i], sq); }
    } else {
      *(u32x4*)((char*)out + ((size_t)row * DM + c8 * 8) * 2) = o;
    }
  }
  if (MODE == 0) {
    ss = sum_g(ss); sq = sum_g(sq);
    if (g == 0) ((f32x2*)(ws + WS_ST))[row * 16 + tn] = f32x2{ss, sq};
  }
  STAMPW(2 + 2 * MODE, 2);
}

using CD = Cfg<256, 224, 32, 4, 4, 2, 1, 32, 0, 1>;
DEVI float geglu(float x, float u) {
  constexpr float K1 = -2.f * 0.7978845608028654f * 1.4426950408889634f, K2 = K1 * 0.044715f;
  float e = __builtin_amdgcn_exp2f(x * (K1 + K2 * x * x));
  return x * __builtin_amdgcn_rcpf(1.f + e) * u;
}
__global__ __launch_bounds__(512, 2) void kD(const void* __restrict__ wg, const void* __restrict__ wu, const void* __restrict__ wd, char* __restrict__ ws) {
  using C = CD;
  STAMP(3, 0);
  __shared__ __attribute__((aligned(256))) char sm[C::LDS + 4096];
  unsigned lds0 = (unsigned)(size_t)sm;
  constexpr unsigned O_ROW = C::LDS, O_CS = C::LDS + 2048;
  int tid = threadIdx.x, lane = tid & 63, wave = __builtin_amdgcn_readfirstlane(tid >> 6);
  int wn = wave & 1, wm = wave >> 1;
  int b = blockIdx.x, xcd = b & 7, idx = b >> 3;
  int tm = (xcd >> 2) * 4 + (idx & 3), tn = (xcd & 3) * 8 + (idx >> 2);
  {
    Loader<C> L;
    loader_init<C>(L, lds0, wave, lane, ws + WS_HT, tm * 256, DM, wg, wu, FF, 0, [&](int lc) { return tn * 112 + lc * 8; });
    const char* sp = ws + WS_ST + (size_t)(tm * 256 + wave * 32) * 128 + lane * 16;
    f32x4 sv[4];
#pragma unroll
    for (int i = 0; i < 4; ++i) sv[i] = gload4(sp + i * 1024);
    int ct = tid < 448 ? tid >> 1 : 223;
    const float* cp = (const float*)(ws + WS_CS) + (tid & 1) * 16 * FF + (ct < 112 ? tn * 112 + ct : FF + tn * 112 + ct - 112);
    float cv[8];
#pragma unroll
    for (int i = 0; i < 8; ++i) cv[i] = gload1(cp + i * 2 * FF);
    STAMP3(3, 1);
    prologue<C>(L, wave, lds0);
    STAMP3(3, 2);
    wait_stages<C, C::S - 1>(wave);
    asm volatile("" : "+v"(sv[0]), "+v"(sv[1]), "+v"(sv[2]), "+v"(sv[3]), "+v"(cv[0]), "+v"(cv[1]), "+v"(cv[2]), "+v"(cv[3]), "+v"(cv[4]), "+v"(cv[5]), "+v"(cv[6]), "+v"(cv[7]));
    {
      float s[4], q[4];
#pragma unroll
      for (int i = 0; i < 4; ++i) { s[i] = sv[i][0] + sv[i][2]; q[i] = sv[i][1] + sv[i][3]; }
      bool o1 = lane & 1, o2 = lane & 2;
      float sa = (o1 ? s[1] : s[0]) + __shfl_xor(o1 ? s[0] : s[1], 1), sb = (o1 ? s[3] : s[2]) + __shfl_xor(o1 ? s[2] : s[3], 1);
      float qa = (o1 ? q[1] : q[0]) + __shfl_xor(o1 ? q[0] : q[1], 1), qb = (o1 ? q[3] : q[2]) + __shfl_xor(o1 ? q[2] : q[3], 1);
      float ss = (o2 ? sb : sa) + __shfl_xor(o2 ? sa : sb, 2), qq = (o2 ? qb : qa) + __shfl_xor(o2 ? qa : qb, 2);
      ss += __shfl_xor(ss, 4); qq += __shfl_xor(qq, 4);
      float mean = ss * (1.f / DM), var = fmaxf(qq * (1.f / DM) - mean * mean, 0.f);
      float rstd = __builtin_amdgcn_rsqf(var + 1e-5f);
      if (!(lane & 4)) *LDS3(f32x2, lds0 + O_ROW + (wave * 32 + (lane & 3) * 8 + (lane >> 3)) * 8) = f32x2{mean, rstd};
    }
    float cs = ((cv[0] + cv[1]) + (cv[2] + cv[3])) + ((cv[4] + cv[5]) + (cv[6] + cv[7]));
    cs += __shfl_xor(cs, 1);
    if (tid < 448 && !(tid & 1)) *LDS3(float, lds0 + O_CS + ct * 4) = cs;
    RdAddr<C> R; rd_init<C>(R, lds0, wm, wn, 0, lane, 0, 0, 0);
    f32x4 acc[4][7];
#pragma unroll
    for (int m = 0; m < 4; ++m)
#pragma unroll
      for (int j = 0; j < 7; ++j) acc[m][j] = f32x4{0, 0, 0, 0};
    PfHook<1> hk; hk.v = lane * 64; hk.l = lds0 + C::LDS + 3072; hk.p = uni_ptr((const char*)wd + (size_t)b * 28672 + (wave < 7 ? wave : 6) * 4096);
    hk.k1 = wave < 7 ? 4 + 3 * wave : -1;
    mainloop<C, 0x7fu>(acc, L, R, wave, lds0, hk);
    STAMP(3, 1);
    int g = lane >> 4, r15 = lane & 15, hi = g >> 1;
    f32x2 st[2];
#pragma unroll
    for (int p = 0; p < 2; ++p) st[p] = *LDS3(f32x2, lds0 + O_ROW + (wm * 64 + (p * 2 + hi) * 16 + r15) * 8);
    int smf = tm * 16 + wm * 4 + ((g & 1) * 2 + hi);
    u32x4* dst = (u32x4*)(ws + WS_ACT) + ((size_t)smf * (FF / 8) + tn * 14 + wn * 7) * 16 + r15;
    const __amdgpu_buffer_rsrc_t drs = __builtin_amdgcn_make_buffer_rsrc(ws + WS_ACT, 0, SEQ * FF * 2, 0x00020000);
    unsigned doff = ((smf * (FF / 8) + tn * 14 + wn * 7) * 16 + r15) * 16;
#pragma unroll
    for (int j = 0; j < 7; ++j) {
      unsigned ca = lds0 + O_CS + ((wn * 7 + j) * 8 + (g & 1) * 4) * 4;
      f32x4 cg = *LDS3(f32x4, ca), cu = *LDS3(f32x4, ca + 448);
      unsigned pk[2][2];
#pragma unroll
      for (int p = 0; p < 2; ++p) {
        float a[4];
#pragma unroll
        for (int r = 0; r < 4; ++r) {
          float x = acc[2 * p][j][r], y = acc[2 * p + 1][j][r];
          swap32(x, y);
          a[r] = geglu((x - st[p][0] * cg[r]) * st[p][1], (y - st[p][0] * cu[r]) * st[p][1]);
        }
        pk[p][0] = pack_bf16(a[0], a[1]); pk[p][1] = pack_bf16(a[2], a[3]);
      }
      swap16u(pk[0][0], pk[1][0]); swap16u(pk[0][1], pk[1][1]);
      __builtin_amdgcn_raw_buffer_store_b128(u32x4{pk[0][0], pk[0][1], pk[1][0], pk[1][1]}, drs, doff + j * 256, 0, DST);
    }
  }
  STAMPW(3, 2);
}

using CE = Cfg<128, 128, 64, 4, 2, 2, 2, FF / 128, 0, 0>;
typedef __attribute__((address_space(1))) unsigned gu32;
__global__ __launch_bounds__(512, 2) void kE(const void* __restrict__ w, void* __restrict__ out, char* __restrict__ ws) {
  using C = CE;
  STAMP(4, 0);
  __shared__ __attribute__((aligned(256))) char sm[C::LDS];
  unsigned lds0 = (unsigned)(size_t)sm;
  int tid = threadIdx.x, lane = tid & 63, wave = __builtin_amdgcn_readfirstlane(tid >> 6);
  int wk = wave & 1, wn = (wave >> 1) & 1, wm = wave >> 2;
  int b = blockIdx.x, xcd = b & 7, idx = b >> 3;
  int kh = xcd & 1, tm = (xcd >> 1) * 4 + (idx & 3), tn = idx >> 2;
  int g = lane >> 4, r15 = lane & 15;
  int c8 = tn * 16 + wn * 8 + 2 * (2 * wk + (g & 1)) + (g >> 1);
  int mf0 = tm * 8 + wm * 4 + 2 * kh;
  u32x4 res[2];
#pragma unroll
  for (int m = 0; m < 2; ++m) res[m] = ((const u32x4*)(ws + WS_HT))[((mf0 + m) * 128 + c8) * 16 + r15];
  Loader<C> L;
  loader_init<C>(L, lds0, wave, lane, ws + WS_ACT, tm * 128, FF, w, w, DM, kh * (FF / 2), [&](int lc) { return tn * 128 + lc * 8; }, 2 * kh);
  STAMP3(4, 1);
  prologue<C>(L, wave, lds0);
  STAMP3(4, 2);
  RdAddr<C> R; rd_init<C>(R, lds0, wm, wn, wk, lane, 0, 0, 2 * wk);
  f32x4 acc[4][4];
#pragma unroll
  for (int m = 0; m < 4; ++m)
#pragma unroll
    for (int j = 0; j < 4; ++j) acc[m][j] = f32x4{0, 0, 0, 0};
  NoHook hk;
  mainloop<C, 0xfu>(acc, L, R, wave, lds0, hk);
  STAMP(4, 1);
#pragma unroll
  for (int m = 3; m >= 0; --m)
#pragma unroll
    for (int j = 0; j < 2; ++j) *LDS3(f32x4, lds0 + ((wave * 8 + m * 2 + j) * 64 + lane) * 16) = acc[m][2 + j];
  wait_lgkm0(); bar();
  const __amdgpu_buffer_rsrc_t rs = __builtin_amdgcn_make_buffer_rsrc(ws + WS_XCH, 0, 256 * 8 * 4096, 0x00020000);
#pragma unroll
  for (int m = 3; m >= 0; --m)
#pragma unroll
    for (int j = 0; j < 2; ++j) {
      acc[m][j] += *LDS3(f32x4, lds0 + (((wave ^ 1) * 8 + m * 2 + j) * 64 + lane) * 16);
      if (m >= 2) __builtin_amdgcn_raw_buffer_store_b128(__builtin_bit_cast(u32x4, acc[m][j]), rs, (((b * 8 + wave) * 4 + (m - 2) * 2 + j) * 64 + lane) * 16, 0, 16);
    }
  asm volatile("s_waitcnt vmcnt(0)" ::: "memory");
  if (lane == 0) __hip_atomic_store((gu32*)(ws + WS_FLG + (b * 8 + wave) * 128), 1u, __ATOMIC_RELAXED, __HIP_MEMORY_SCOPE_AGENT);
  {
    gu32* pf = (gu32*)(ws + WS_FLG + ((b ^ 1) * 8 + wave) * 128);
    for (int spin = 0; spin < (1 << 22); ++spin) {
      if (__builtin_amdgcn_readfirstlane(__hip_atomic_load(pf, __ATOMIC_RELAXED, __HIP_MEMORY_SCOPE_AGENT)) == 1u) break;
      __builtin_amdgcn_s_sleep(1);
    }
    asm volatile("" ::: "memory");
  }
  u32x4 x[4];
#pragma unroll
  for (int i = 0; i < 4; ++i) x[i] = __builtin_amdgcn_raw_buffer_load_b128(rs, ((((b ^ 1) * 8 + wave) * 4 + i) * 64 + lane) * 16, 0, 16);
#pragma unroll
  for (int m = 0; m < 2; ++m) {
    f32x4 a = acc[m][0] + __builtin_bit_cast(f32x4, x[2 * m]), c = acc[m][1] + __builtin_bit_cast(f32x4, x[2 * m + 1]);
#pragma unroll
    for (int r = 0; r < 4; ++r) { float u = a[r], v = c[r]; swap16(u, v); a[r] = u; c[r] = v; }
    u32x4 o;
    o[0] = pack_bf16(a[0] + bf_lo(res[m][0]), a[1] + bf_hi(res[m][0]));
    o[1] = pack_bf16(a[2] + bf_lo(res[m][1]), a[3] + bf_hi(res[m][1]));
    o[2] = pack_bf16(c[0] + bf_lo(res[m][2]), c[1] + bf_hi(res[m][2]));
    o[3] = pack_bf16(c[2] + bf_lo(res[m][3]), c[3] + bf_hi(res[m][3]));
    st16<(WTM & 8) != 0>(out, 4 * MB, (((mf0 + m) * 16 + r15) * DM + c8 * 8) * 2, o);
  }
  STAMPW(4, 2);
}

extern "C" void kernel_launch(void* const* d_in, const int* in_sizes, int n_in, void* d_out, int out_size, void* d_ws, size_t ws_size,
                              hipStream_t stream) {
  char* ws = (char*)d_ws;
  kA<<<256, 512, 0, stream>>>(d_in[0], d_in[1], d_in[2], d_in[4], d_in[5], d_in[6], ws);
  kB<<<CSA ? 256 : 512, 256, 0, stream>>>(d_in[4], d_in[5], ws);
  kR<0><<<256, 512, 0, stream>>>(d_in[2], d_in[0], nullptr, ws);
  kD<<<256, 512, 0, stream>>>(d_in[4], d_in[5], d_in[6], ws);
  kE<<<256, 512, 0, stream>>>(d_in[6], d_out, ws);
}
```

```cpp
#define SV 0
#include <hip/hip_runtime.h>
#include <stdint.h>
#define STAGE_D 0
#define UMAP 1
#define STAG 2

typedef __attribute__((ext_vector_type(4))) short s16x4;
typedef __attribute__((ext_vector_type(8))) short s16x8;
typedef __attribute__((ext_vector_type(8))) __bf16 bf16x8;
typedef __attribute__((ext_vector_type(2))) __bf16 bf16x2;
typedef __attribute__((ext_vector_type(4))) float f32x4;
typedef __attribute__((ext_vector_type(2))) float f32x2;
typedef __attribute__((ext_vector_type(4))) unsigned u32x4;
typedef __attribute__((ext_vector_type(2))) unsigned u32x2;
#define LDS3(T, a) ((__attribute__((address_space(3))) T*)(size_t)(a))
#define DEVI __device__ __forceinline__

template <int N> DEVI void wait_vm() { asm volatile("s_waitcnt vmcnt(%0)" ::"n"(N) : "memory"); }
DEVI void wait_lgkm0() { asm volatile("s_waitcnt lgkmcnt(0)" ::: "memory"); }
DEVI void bar() { __builtin_amdgcn_s_barrier(); }
DEVI void glds16(unsigned voff, const char* sbase, unsigned lds) {
  asm volatile("s_mov_b32 m0, %2\n\ts_nop 0\n\tglobal_load_lds_dwordx4 %0, %1" ::"v"(voff), "s"(sbase), "s"(lds) : "memory");
}
DEVI bf16x8 lds_b128(unsigned a) { return *LDS3(bf16x8, a); }
DEVI s16x4 lds_tr(unsigned a) { return __builtin_amdgcn_ds_read_tr16_b64_v4i16(LDS3(s16x4, a)); }
DEVI unsigned pack_bf16(float lo, float hi) {
  bf16x2 v; v[0] = (__bf16)lo; v[1] = (__bf16)hi; return __builtin_bit_cast(unsigned, v);
}
DEVI float bf_lo(unsigned u) { return __uint_as_float(u << 16); }
DEVI float bf_hi(unsigned u) { return __uint_as_float(u & 0xffff0000u); }
DEVI float dot2(unsigned a, unsigned b, float c) {
  return __builtin_amdgcn_fdot2_f32_bf16(__builtin_bit_cast(bf16x2, a), __builtin_bit_cast(bf16x2, b), c, false);
}
DEVI void swap16(float& a, float& b) {
  auto r = __builtin_amdgcn_permlane16_swap(__float_as_uint(a), __float_as_uint(b), false, false);
  a = __uint_as_float(r[0]); b = __uint_as_float(r[1]);
}
DEVI void swap16u(unsigned& a, unsigned& b) {
  auto r = __builtin_amdgcn_permlane16_swap(a, b, false, false);
  a = r[0]; b = r[1];
}
DEVI void swap32(float& a, float& b) {
  auto r = __builtin_amdgcn_permlane32_swap(__float_as_uint(a), __float_as_uint(b), false, false);
  a = __uint_as_float(r[0]); b = __uint_as_float(r[1]);
}
DEVI float sum_g(float x) {
  float a = x, b = x; swap16(a, b); x = a + b;
  a = x; b = x; swap32(a, b); return a + b;
}
DEVI f32x4 mfma16(bf16x8 a, bf16x8 b, f32x4 c) { return __builtin_amdgcn_mfma_f32_16x16x32_bf16(a, b, c, 0, 0, 0); }

template <int BM_, int BN_, int BK_, int S_, int WM_, int WN_, int WK_, int NK_, int AMODE_, int BMODE_, int NPF_ = 0>
struct Cfg {
  static constexpr int NPF = NPF_;
  static constexpr int BM = BM_, BN = BN_, BK = BK_, S = S_, WM = WM_, WN = WN_, WK = WK_, NK = NK_, AMODE = AMODE_, BMODE = BMODE_;
  static constexpr int NW = WM * WN * WK;
  static constexpr int MF = BM / WM / 16, NF = BN / WN / 16, KF = BK / 32, KFW = KF / WK;
  static constexpr int A_BYTES = BM * BK * 2;
  static constexpr int B_BYTES = BN * BK * 2 + (BMODE == 1 ? 256 : 0);
  static constexpr int STAGE = A_BYTES + B_BYTES;
  static constexpr int PA = A_BYTES / 1024, PB = BN * BK * 2 / 1024, P = PA + PB;
  static constexpr int NLW = NPF ? NW - 1 : NW;
  static constexpr int NP = (P + NLW - 1) / NLW;
  static constexpr int FULLW = P - (NP - 1) * NLW;
  static constexpr int RS = (BMODE == 1 ? BN : BN * 2);
  static constexpr int LDS = S * STAGE;
  static constexpr bool SLOT = (MF == 2);
};

template <class C> struct Frags { bf16x8 a[C::KFW][C::MF]; bf16x8 b[C::KFW][C::NF]; };
template <class C> struct Loader {
  const char* sbase[C::NP]; unsigned voff[C::NP]; unsigned ldso[C::NP]; unsigned sstr[C::NP];
  const char* pfb[4]; unsigned pfv, pfl, pfs[4];
};
DEVI void glds4(unsigned voff, const char* sbase, unsigned lds) {
  asm volatile("s_mov_b32 m0, %2\n\ts_nop 0\n\tglobal_load_lds_dword %0, %1" ::"v"(voff), "s"(sbase), "s"(lds) : "memory");
}
template <class C> DEVI void issue_pf(Loader<C>& L, int i) { glds4(L.pfv, L.pfb[i], L.pfl); L.pfb[i] += L.pfs[i]; }
DEVI const char* uni_ptr(const char* p) {
  unsigned long long u = (unsigned long long)p;
  unsigned lo = __builtin_amdgcn_readfirstlane((unsigned)u), hi = __builtin_amdgcn_readfirstlane((unsigned)(u >> 32));
  return (const char*)(((unsigned long long)hi << 32) | lo);
}

template <class C> DEVI void issue_piece(Loader<C>& L, int i, int wave, unsigned bufbase) {
  if (i < C::NP - 1 ? (C::NLW == C::NW || wave < C::NLW) : (C::FULLW == C::NW || wave < C::FULLW)) glds16(L.voff[i], L.sbase[i], bufbase + L.ldso[i]);
  L.sbase[i] += L.sstr[i];
}
template <class C, class ColMap>
DEVI void loader_init(Loader<C>& L, unsigned lds0, int wave, int lane, const void* A, int m0, int lda, const void* B0, const void* B1, int ldb, int k0,
                      ColMap colmap, int mxor = 0) {
#pragma unroll
  for (int i = 0; i < C::NP; ++i) {
    int p = wave + i * C::NLW;
    if (p >= C::P) p = C::P - 1;
    if (p < C::PA) {
      if (C::AMODE == 0) {
        int mb = p / C::KF, kb = p % C::KF;
        L.sbase[i] = (const char*)A + ((size_t)(m0 / 16 + (mb ^ mxor)) * (lda / 8) + (k0 / 8 + kb * 4)) * 256;
        L.voff[i] = lane * 16;
        L.sstr[i] = C::BK / 8 * 256;
      } else {
        int row = 8 * p + (lane >> 3), pos = lane & 7, lc = pos ^ ((row >> 1) & 7);
        L.sbase[i] = (const char*)A + ((size_t)(m0 + 8 * p) * lda + k0) * 2;
        L.voff[i] = ((lane >> 3) * lda + lc * 8) * 2;
        L.sstr[i] = C::BK * 2;
      }
      L.ldso[i] = p * 1024;
    } else {
      int pb = p - C::PA;
      if (C::BMODE == 0) {
        constexpr int CH = C::BN / 8;
        int ci = pb * 64 + lane, k = ci / CH, c = ci % CH, u = c >> 1;
        int sg = (C::RS % 256 == 0) ? ((k & 3) | (((k >> 3) & 1) << 2)) : (C::RS % 256 == 128) ? (((k >> 1) & 1) | (((k >> 3) & 1) << 1)) : ((k >> 3) & 1);
        int lc = ((u ^ sg) << 1) | (c & 1);
        L.sbase[i] = (const char*)B0 + (size_t)k0 * ldb * 2;
        L.voff[i] = (k * ldb + colmap(lc)) * 2;
        L.ldso[i] = C::A_BYTES + pb * 1024;
      } else {
        constexpr int CH = C::BN / 16, HP = C::PB / 2;
        int half = pb / HP, pp = pb % HP;
        int ci = pp * 64 + lane, k = ci / CH, c = ci % CH, lc = c ^ ((k >> 3) & 1);
        L.sbase[i] = (const char*)(half ? B1 : B0) + (size_t)k0 * ldb * 2;
        L.voff[i] = (k * ldb + colmap(lc)) * 2;
        L.ldso[i] = C::A_BYTES + half * (HP * 1024 + 128) + pp * 1024;
      }
      L.sstr[i] = C::BK * ldb * 2;
    }
    {
      unsigned long long u = (unsigned long long)L.sbase[i];
      unsigned lo = __builtin_amdgcn_readfirstlane((unsigned)u), hi = __builtin_amdgcn_readfirstlane((unsigned)(u >> 32));
      L.sbase[i] = (const char*)(((unsigned long long)hi << 32) | lo);
      L.ldso[i] = __builtin_amdgcn_readfirstlane(L.ldso[i]);
      L.sstr[i] = __builtin_amdgcn_readfirstlane(L.sstr[i]);
    }
    issue_piece<C>(L, i, wave, lds0);
  }
}

template <class C, int NST, int X = 0> DEVI void wait_stages(int wave) {
  if (C::FULLW == C::NW || wave < C::FULLW) wait_vm<NST * C::NP + X>();
  else if (C::NLW == C::NW || wave < C::NLW) wait_vm<NST*(C::NP - 1) + X>();
}

template <class C> struct RdAddr { unsigned a[C::KFW][2]; unsigned b[C::NF]; unsigned kbo[2]; };
template <class C> DEVI void rd_init(RdAddr<C>& R, unsigned lds0, int wm, int wn, int wk, int lane, int mx, int kx, int jx) {
  int g = lane >> 4, r15 = lane & 15, q = (lane & 15) >> 2, p = lane & 3;
#pragma unroll
  for (int kb = 0; kb < C::KFW; ++kb)
#pragma unroll
    for (int m = 0; m < 2; ++m) {
      int ka = wk * C::KFW + (kb ^ kx), ma = wm * C::MF + (m ^ mx);
      if (C::AMODE == 0) R.a[kb][m] = lds0 + (ma * C::KF + ka) * 1024 + lane * 16;
      else R.a[kb][m] = lds0 + (ma * 16 + r15) * 128 + (((ka * 4 + g) ^ ((r15 >> 1) & 7)) * 16);
    }
  R.kbo[0] = kx * 32 * C::RS; R.kbo[1] = (1 ^ kx) * 32 * C::RS;
  int krow = wk * C::KFW * 32 + 8 * g + q;
#pragma unroll
  for (int j = 0; j < C::NF; ++j) {
    int U = wn * C::NF + (j ^ jx);
    if (UMAP && C::AMODE == 1) U = (j >> 1) * 4 + wn * 2 + ((j ^ jx) & 1);
    if (C::BMODE == 0) {
      int sg = (C::RS % 256 == 0) ? (q | ((g & 1) << 2)) : (C::RS % 256 == 128) ? ((q >> 1) | ((g & 1) << 1)) : (g & 1);
      R.b[j] = lds0 + C::A_BYTES + krow * C::RS + ((U ^ sg) * 32) + p * 8;
    } else {
      R.b[j] = lds0 + C::A_BYTES + (p >> 1) * (C::PB / 2 * 1024 + 128) + krow * C::RS + ((U ^ (g & 1)) * 16) + (p & 1) * 8;
    }
  }
}
template <class C> DEVI void read_a(Frags<C>& f, const RdAddr<C>& R, unsigned so, int kb, int mf) {
  if (C::SLOT) f.a[kb][mf] = lds_b128(R.a[kb][mf & 1] + so);
  else f.a[kb][mf] = lds_b128(R.a[0][0] + so + (mf * C::KF + kb) * 1024);
}
template <class C> DEVI void read_b(Frags<C>& f, const RdAddr<C>& R, unsigned so, int kb, int j) {
  unsigned a = (C::AMODE == 1) ? R.b[j] + (so + R.kbo[kb & 1]) : R.b[j] + so + (kb * 32) * C::RS;
  s16x4 lo = lds_tr(a);
  s16x4 hi = lds_tr(a + 4 * C::RS);
  s16x8 v = __builtin_shufflevector(lo, hi, 0, 1, 2, 3, 4, 5, 6, 7);
  f.b[kb][j] = __builtin_bit_cast(bf16x8, v);
}
template <class C> DEVI void read_all(Frags<C>& f, const RdAddr<C>& R, unsigned so) {
#pragma unroll
  for (int kb = 0; kb < C::KFW; ++kb) {
#pragma unroll
    for (int m = 0; m < C::MF; ++m) read_a<C>(f, R, so, kb, m);
#pragma unroll
    for (int j = 0; j < C::NF; ++j) read_b<C>(f, R, so, kb, j);
  }
}

template <class C, unsigned SWAPMASK, bool ISSUE, bool READ, int NOUT, int PF, class Hook>
DEVI void iter(f32x4 (&acc)[C::MF][C::NF], const Frags<C>& cur, Frags<C>& nxt, Loader<C>& L, const RdAddr<C>& R, int wave, unsigned lds0,
               unsigned& bcur, Hook& hook, int kt) {
  wait_lgkm0();
  if (READ) { wait_stages<C, NOUT, (PF == 10 ? Hook::NT : 0)>(wave); }
  bar();
  if (PF == 9) hook.tail();
  unsigned bn = bcur + C::STAGE; if (bn == C::LDS) bn = 0;
  constexpr int NM = C::KFW * C::MF * C::NF;
  constexpr int NR = C::KFW * (C::MF + C::NF);
  constexpr int SEG = C::NP + 1;
#pragma unroll
  for (int s = 0; s < SEG; ++s) {
    if (READ) {
#pragma unroll
      for (int r = s * NR / SEG; r < (s + 1) * NR / SEG; ++r) {
        int kb = r / (C::MF + C::NF), x = r % (C::MF + C::NF);
        if (x < C::MF) read_a<C>(nxt, R, bn, kb, x); else read_b<C>(nxt, R, bn, kb, x - C::MF);
      }
    }
#pragma unroll
    for (int i = s * NM / SEG; i < (s + 1) * NM / SEG; ++i) {
      int kb = i / (C::MF * C::NF), x = i % (C::MF * C::NF), j = x / C::MF, m = x % C::MF;
      if ((SWAPMASK >> j) & 1) acc[m][j] = mfma16(cur.b[kb][j], cur.a[kb][m], acc[m][j]);
      else acc[m][j] = mfma16(cur.a[kb][m], cur.b[kb][j], acc[m][j]);
    }
    hook.seg(cur, s, SEG);
#pragma unroll
    for (int x = 0; x < 8; ++x) {
      __builtin_amdgcn_sched_group_barrier(0x8, 1, 0);
      __builtin_amdgcn_sched_group_barrier(0x100, 1, 0);
      if (Hook::VALU) __builtin_amdgcn_sched_group_barrier(0x2, Hook::VALU, 0);
    }
    __builtin_amdgcn_sched_barrier(0);
    if (PF && PF < 9 && (s == 1 || s == 3) && (PF - 1) * 2 + (s >> 1) < C::NPF) { if (wave == C::NW - 1) issue_pf<C>(L, (PF - 1) * 2 + (s >> 1)); }
    if (ISSUE && s < C::NP) { issue_piece<C>(L, s, wave, lds0 + bcur); __builtin_amdgcn_sched_barrier(0); }
    if (s == SEG - 1) hook.mid(kt);
  }
  bcur = bn;
}

template <class C> DEVI void prologue(Loader<C>& L, int wave, unsigned lds0) {
#pragma unroll
  for (int s = 1; s < C::S; ++s) {
#pragma unroll
    for (int i = 0; i < C::NP; ++i) issue_piece<C>(L, i, wave, lds0 + s * C::STAGE);
  }
}

template <class C, unsigned SWAPMASK, class Hook>
DEVI void mainloop(f32x4 (&acc)[C::MF][C::NF], Loader<C>& L, const RdAddr<C>& R, int wave, unsigned lds0, Hook& hook) {
  constexpr int S = C::S, NK = C::NK;
  Frags<C> f0, f1;
  wait_stages<C, S - 1>(wave);
  bar();
  read_all<C>(f0, R, 0);
  unsigned bcur = 0;
  constexpr int NMAIN = NK - S;
  constexpr int NPAIR = NMAIN / 2;
  for (int it = 0; it < NPAIR; ++it) {
    iter<C, SWAPMASK, true, true, S - 2, (C::NPF > 0 ? 1 : 0)>(acc, f0, f1, L, R, wave, lds0, bcur, hook, 2 * it);
    iter<C, SWAPMASK, true, true, S - 2, (C::NPF > 2 ? 2 : 0)>(acc, f1, f0, L, R, wave, lds0, bcur, hook, 2 * it + 1);
  }
  constexpr int T0 = 2 * NPAIR;
#define TAILIT(T, CUR, NXT)                                                        \
  if constexpr (T0 + (T) < NK) {                                                   \
    constexpr int kt = T0 + (T);                                                   \
    constexpr bool iss = kt + S < NK, rd = kt + 1 < NK;                            \
    constexpr int last_issued = (kt + S - 1 < NK - 1) ? kt + S - 1 : NK - 1;       \
    constexpr int nout = rd ? last_issued - (kt + 1) : 0;                          \
    iter<C, SWAPMASK, iss, rd, nout, (kt == NK - S ? 9 : kt > NK - S ? 10 : 0)>(acc, CUR, NXT, L, R, wave, lds0, bcur, hook, kt); \
  }
  TAILIT(0, f0, f1) TAILIT(1, f1, f0) TAILIT(2, f0, f1) TAILIT(3, f1, f0) TAILIT(4, f0, f1) TAILIT(5, f1, f0) TAILIT(6, f0, f1) TAILIT(7, f1, f0)
#undef TAILIT
}
struct NoHook { static constexpr int VALU = 0, NT = 0; template <class F> DEVI void seg(const F&, int, int) const {} DEVI void tail() const {} DEVI void mid(int) const {} };
template <int N> struct PfHook {
  static constexpr int VALU = 0, NT = (STAGE_D ? 0 : N);
  const char* p; unsigned v, l; int k1;
  template <class F> DEVI void seg(const F&, int, int) const {}
  DEVI void tail() const {
#pragma unroll
    for (int i = 0; i < N; ++i) glds4(v, p + i * 4096, l);
  }
  DEVI void mid(int kt) const {
  }
};

#define TNX 0
#define LINPF 2
#define DST 16
#define CSA 1
#define STAGD 4
#define STAG0 3
constexpr int SEQ = 2048, DM = 1024, FF = 3584;
constexpr size_t MB = 1u << 20;
constexpr size_t WS_QF = 0;
constexpr size_t WS_OT = 4 * MB;
constexpr size_t WS_HT = 8 * MB;
constexpr size_t WS_KVP = 12 * MB;
constexpr size_t WS_ZP = 14 * MB;
constexpr size_t WS_ST = 14 * MB + 256 * 1024;
constexpr size_t WS_CS = 14 * MB + 768 * 1024;
constexpr size_t WS_ACT = 16 * MB;
constexpr size_t WS_FLG = 15 * MB + 512 * 1024;
constexpr size_t WS_XCH = 32 * MB;
#define DIAG3 0
#define STAMP3(kid, pos)
#define STAMP(kid, i)
#define STAMPW(kid, i)
constexpr unsigned ONES2 = 0x3f803f80u;
#define WTM 15
template <bool WT> DEVI void st16(void* base, unsigned bytes, unsigned off, u32x4 v) {
  if (WT) __builtin_amdgcn_raw_buffer_store_b128(v, __builtin_amdgcn_make_buffer_rsrc(base, 0, bytes, 0x00020000), off, 0, 16);
  else *(u32x4*)((char*)base + off) = v;
}

DEVI f32x4 gload4(const void* p) { f32x4 r; asm volatile("global_load_dwordx4 %0, %1, off" : "=v"(r) : "v"(p) : "memory"); return r; }
DEVI float gload1(const void* p) { float r; asm volatile("global_load_dword %0, %1, off" : "=v"(r) : "v"(p) : "memory"); return r; }
DEVI float phi(float t) { return (t > 0.f ? t + 1.f : __expf(t)) + 1e-6f; }

#define SVV 1
#define SA 3
using CA = Cfg<128, 192, 64, SA, 4, 2, 1, 16, 1, 0, 0>;
DEVI void touch(unsigned& d, unsigned voff, const char* sbase, int a, int b) {
  asm volatile("s_cmp_lg_u32 %3, %4\n\ts_cbranch_scc1 .Ltch%=\n\tglobal_load_dword %0, %1, %2\n.Ltch%=:" : "+v"(d) : "v"(voff), "s"(sbase), "s"(a), "s"(b) : "memory", "scc");
}
struct HookA {
  static constexpr int VALU = SV ? SVV : 0, NT = 1;
  f32x4 rs, rq, c[3];
  const char* p; unsigned v, v2, d; int k1;
  DEVI void tail() { touch(d, v2, p, k1, k1); }
  DEVI void mid(int kt) {
    if (kt < CA::NK - CA::S) touch(d, v, p, kt, k1);
  }
  template <bool V> DEVI void acc1(f32x4& r, bf16x8 x, bool sqr) {
    bf16x8 ones = __builtin_bit_cast(bf16x8, u32x4{ONES2, ONES2, ONES2, ONES2});
    if (V) {
      u32x4 u = __builtin_bit_cast(u32x4, x);
#pragma unroll
      for (int i = 0; i < 4; ++i) r[i & 1] = dot2(u[i], sqr ? u[i] : ONES2, r[i & 1]);
    } else r = mfma16(sqr ? x : ones, x, r);
  }
  template <class F> DEVI void seg(const F& f, int s, int SEG) {
#pragma unroll
    for (int u = s * 7 / SEG; u < (s + 1) * 7 / SEG; ++u) {
      if (u == 0) acc1<(SV & 1) != 0>(rs, f.a[0][0], false);
      if (u == 1) acc1<(SV & 2) != 0>(rq, f.a[0][0], true);
      if (u == 2) acc1<(SV & 4) != 0>(c[0], f.b[0][0], false);
      if (u == 3) acc1<(SV & 4) != 0>(c[1], f.b[0][2], false);
      if (u == 4) acc1<(SV & 4) != 0>(c[2], f.b[0][4], false);
      if (u == 5) acc1<(SV & 1) != 0>(rs, f.a[1][0], false);
      if (u == 6) acc1<(SV & 2) != 0>(rq, f.a[1][0], true);
    }
  }
};

__global__ __launch_bounds__(512, 2) void kA(const void* __restrict__ x, const void* __restrict__ wqkv, const void* __restrict__ wo, const void* __restrict__ wg,
                                             const void* __restrict__ wu, const void* __restrict__ wd, char* __restrict__ ws) {
  using C = CA;
  STAMP(0, 0);
  __shared__ __attribute__((aligned(256))) char sm[C::LDS];
  unsigned lds0 = (unsigned)(size_t)sm;
  int tid = threadIdx.x, lane = tid & 63, wave = __builtin_amdgcn_readfirstlane(tid >> 6);
  int wn = wave & 1, wm = wave >> 1;
  int b = blockIdx.x, xcd = b & 7, idx = b >> 3;
  int tm = (xcd >> 1) * 4 + (idx & 3), tn = (xcd & 1) * 8 + ((idx >> 2) ^ TNX);
  Loader<C> L;
  loader_init<C>(L, lds0, wave, lane, x, tm * 128, DM, wqkv, wqkv, 3 * DM, 0, [&](int lc) {
    return (lc >> 3) * DM + tn * 64 + (lc & 7) * 8;
  });
  STAMP3(0, 1);
  prologue<C>(L, wave, lds0);
  STAMP3(0, 2);
  HookA hk; hk.d = 0;
  touch(hk.d, lane * 64, uni_ptr((const char*)wqkv + (size_t)b * 24576 + (wave < 6 ? wave : 0) * 4096), (wave + 2) >> 3, 0);
  int mx = wn, kx = wm & 1, jx = wm >> 1;
  RdAddr<C> R; rd_init<C>(R, lds0, wm, wn, 0, lane, mx, kx, jx);
  f32x4 acc[2][6];
#pragma unroll
  for (int m = 0; m < 2; ++m)
#pragma unroll
    for (int j = 0; j < 6; ++j) acc[m][j] = f32x4{0, 0, 0, 0};
  const char* wp = uni_ptr((const char*)(b >> 7 ? wu : wg) + (size_t)((b >> 3) & 15) * 64 * FF * 2 + (b & 7) * 896);
  {
    int i1 = wave * 128 + lane, i2 = i1 + 64;
    hk.p = wave < 7 ? wp : uni_ptr((const char*)wo + (size_t)b * 8192);
    hk.v = wave < 7 ? (i1 / 14) * (FF * 2) + (i1 % 14) * 64 : lane * 64;
    hk.v2 = wave < 7 ? (i2 / 14) * (FF * 2) + (i2 % 14) * 64 : lane * 64 + 4096;
    hk.k1 = STAG0 + wave;
  }
  hk.rs = hk.rq = hk.c[0] = hk.c[1] = hk.c[2] = f32x4{0, 0, 0, 0};
  mainloop<C, 0x3u>(acc, L, R, wave, lds0, hk);
  STAMP(0, 1);
  u32x4 wv[8];
  if (CSA) {
    int l56 = lane < 56 ? lane : 55;
#pragma unroll
    for (int i = 0; i < 8; ++i) wv[i] = *(const __attribute__((address_space(1))) u32x4*)(size_t)(wp + (wave * 8 + i) * (FF * 2) + l56 * 16);
  }
  int g = lane >> 4, r15 = lane & 15;
  constexpr unsigned O_ROW = 0, O_CS = 1024, O_Z = 4096, O_RED = 8192, O_WP = 40960;
  {
    int rr = r15 & 3;
    float s = hk.rs[0], q = rr == 0 ? hk.rq[0] : rr == 1 ? hk.rq[1] : rr == 2 ? hk.rq[2] : hk.rq[3];
    if (SV & 1) s = sum_g(hk.rs[0] + hk.rs[1]);
    if (SV & 2) q = sum_g(hk.rq[0] + hk.rq[1]);
    if (SV & 4) {
#pragma unroll
      for (int i = 0; i < 3; ++i) hk.c[i][0] = sum_g(hk.c[i][0] + hk.c[i][1]);
    }
    float mean = s * (1.f / DM), var = fmaxf(q * (1.f / DM) - mean * mean, 0.f);
    float rstd = __builtin_amdgcn_rsqf(var + 1e-5f);
    if (g == (r15 >> 2)) *LDS3(f32x2, lds0 + O_ROW + (wm * 32 + wn * 16 + r15) * 8) = f32x2{mean, rstd};
    float cc = g == 1 ? hk.c[0][0] : g == 2 ? hk.c[1][0] : hk.c[2][0];
    if (g) *LDS3(float, lds0 + O_CS + (kx * 192 + wn * 96 + (((g - 1) * 2) ^ jx) * 16 + r15) * 4) = cc;
  }
  wait_lgkm0(); bar();
  int head = 2 * tn + wn;
#pragma unroll
  for (int m = 0; m < 2; ++m) {
    int ma = m ^ mx;
    f32x2 st = *LDS3(f32x2, lds0 + O_ROW + (wm * 32 + ma * 16 + r15) * 8);
    unsigned pk[2][2];
#pragma unroll
    for (int j = 0; j < 2; ++j) {
      unsigned ca = lds0 + O_CS + (wn * 96 + (j ^ jx) * 16 + 4 * g) * 4;
      f32x4 cs = *LDS3(f32x4, ca) + *LDS3(f32x4, ca + 768);
      float v[4];
#pragma unroll
      for (int r = 0; r < 4; ++r) v[r] = phi((acc[m][j][r] - st[0] * cs[r]) * st[1]);
      pk[j][0] = pack_bf16(v[0], v[1]); pk[j][1] = pack_bf16(v[2], v[3]);
    }
    u32x4 o = jx ? u32x4{pk[1][0], pk[1][1], pk[0][0], pk[0][1]} : u32x4{pk[0][0], pk[0][1], pk[1][0], pk[1][1]};
    st16<WTM & 1>(ws + WS_QF, 4 * MB, (((tm * 8 + wm * 2 + ma) * 32 + head) * 64 + lane) * 16, o);
  }
  {
    f32x4 sa[2][2];
#pragma unroll
    for (int m = 0; m < 2; ++m) {
      unsigned ra = lds0 + O_ROW + (wm * 32 + (m ^ mx) * 16 + 4 * g) * 8;
      sa[m][0] = *LDS3(f32x4, ra); sa[m][1] = *LDS3(f32x4, ra + 16);
    }
    bf16x8 fr[4];
#pragma unroll
    for (int j = 2; j < 6; ++j) {
      unsigned ca = lds0 + O_CS + (wn * 96 + (j ^ jx) * 16 + r15) * 4;
      float cs = *LDS3(float, ca) + *LDS3(float, ca + 768);
      u32x4 u;
#pragma unroll
      for (int m = 0; m < 2; ++m) {
        float v[4];
#pragma unroll
        for (int r = 0; r < 4; ++r) {
          float t = (acc[m][j][r] - sa[m][r >> 1][(r & 1) * 2] * cs) * sa[m][r >> 1][(r & 1) * 2 + 1];
          v[r] = j < 4 ? phi(t) : t;
        }
        u[m * 2] = pack_bf16(v[0], v[1]); u[m * 2 + 1] = pack_bf16(v[2], v[3]);
      }
      fr[j - 2] = __builtin_bit_cast(bf16x8, u);
    }
    bf16x8 ones = __builtin_bit_cast(bf16x8, u32x4{ONES2, ONES2, ONES2, ONES2});
    f32x4 zero = {0, 0, 0, 0};
#pragma unroll
    for (int fd = 0; fd < 2; ++fd) {
#pragma unroll
      for (int fe = 0; fe < 2; ++fe) {
        f32x4 kv = mfma16(fr[fd], fr[2 + fe], zero);
        *LDS3(f32x4, lds0 + O_RED + ((wave * 4 + ((fd ^ jx) * 2 + (fe ^ jx))) * 64 + lane) * 16) = kv;
      }
      f32x4 z = mfma16(fr[fd], ones, zero);
      if (r15 == 0) *LDS3(f32x4, lds0 + O_Z + ((wave * 2 + (fd ^ jx)) * 4 + g) * 16) = z;
    }
  }
  if (CSA) {
    f32x4 a[2];
#pragma unroll
    for (int c = 0; c < 4; ++c) {
      float lo = 0, hi = 0;
#pragma unroll
      for (int i = 0; i < 8; ++i) { lo += bf_lo(wv[i][c]); hi += bf_hi(wv[i][c]); }
      a[c >> 1][(c & 1) * 2] = lo; a[c >> 1][(c & 1) * 2 + 1] = hi;
    }
    *LDS3(f32x4, lds0 + O_WP + wave * 2048 + lane * 32) = a[0];
    *LDS3(f32x4, lds0 + O_WP + wave * 2048 + lane * 32 + 16) = a[1];
    asm volatile("" ::"v"(hk.d), "v"(a[0]), "v"(a[1]));
  }
  wait_lgkm0(); bar();
  if (CSA && tid >= 384 && tid < 496) {
    int t = tid - 384;
    f32x4 s = *LDS3(f32x4, lds0 + O_WP + t * 16);
#pragma unroll
    for (int k = 1; k < 8; ++k) s += *LDS3(f32x4, lds0 + O_WP + k * 2048 + t * 16);
    st16<WTM & 1>(ws + WS_CS, 16 * 2 * FF * 4, (((b >> 3) & 15) * 2 * FF + (b >> 7) * FF + (b & 7) * 448 + t * 4) * 4, __builtin_bit_cast(u32x4, s));
  }
  {
    int w2 = tid >> 8, i = tid & 255;
    f32x4 s = *LDS3(f32x4, lds0 + O_RED + (w2 * 256 + i) * 16);
#pragma unroll
    for (int k = 1; k < 4; ++k) s += *LDS3(f32x4, lds0 + O_RED + ((k * 2 + w2) * 256 + i) * 16);
    st16<WTM & 1>(ws + WS_KVP, 2 * MB, (((2 * tn + w2) * 16 + tm) * 256 + i) * 16, __builtin_bit_cast(u32x4, s));
    if (tid < 16) {
      int w3 = tid >> 3, c = tid & 7;
      f32x4 z = *LDS3(f32x4, lds0 + O_Z + (w3 * 8 + c) * 16);
#pragma unroll
      for (int k = 1; k < 4; ++k) z += *LDS3(f32x4, lds0 + O_Z + ((k * 2 + w3) * 8 + c) * 16);
      ((f32x4*)(ws + WS_ZP))[((2 * tn + w3) * 16 + tm) * 8 + c] = z;
    }
  }
  STAMPW(0, 2);
}

__global__ __launch_bounds__(256) void kB(const void* __restrict__ wg, const void* __restrict__ wu, char* __restrict__ ws) {
  __shared__ __attribute__((aligned(16))) char sm[8192];
  unsigned lds0 = (unsigned)(size_t)sm;
  int tid = threadIdx.x, lane = tid & 63, wave = tid >> 6, b = blockIdx.x;
  STAMP(1, 0); STAMP(1, 1);
  if (CSA || b < 256) {
    int h = (b & 7) * 4 + ((b >> 3) & 3), rg = b >> 5;
    if (tid < 8) *(unsigned*)(ws + WS_FLG + (b * 8 + tid) * 128) = 0u;
    const f32x4* kvp = (const f32x4*)(ws + WS_KVP) + h * 4096 + tid;
    const f32x4* zp = (const f32x4*)(ws + WS_ZP) + h * 128 + lane;
    f32x4 z0 = gload4(zp), z1 = gload4(zp + 64);
    f32x4 p[16];
#pragma unroll
    for (int i = 0; i < 16; ++i) p[i] = gload4(kvp + i * 256);
    u32x4 q[4];
#pragma unroll
    for (int i = 0; i < 4; ++i) q[i] = __builtin_bit_cast(u32x4, gload4((const u32x4*)(ws + WS_QF) + ((rg * 16 + wave * 4 + i) * 32 + h) * 64 + lane));
    asm volatile("s_waitcnt vmcnt(4)" : "+v"(z0), "+v"(z1), "+v"(p[0]), "+v"(p[1]), "+v"(p[2]), "+v"(p[3]), "+v"(p[4]), "+v"(p[5]), "+v"(p[6]), "+v"(p[7])::"memory");
    asm volatile("" : "+v"(p[8]), "+v"(p[9]), "+v"(p[10]), "+v"(p[11]), "+v"(p[12]), "+v"(p[13]), "+v"(p[14]), "+v"(p[15])::"memory");
    f32x4 zs = z0 + z1;
#pragma unroll
    for (int c = 0; c < 4; ++c) { zs[c] = sum_g(zs[c]); zs[c] += __shfl_xor(zs[c], 8); }
    f32x4 s = ((p[0] + p[1]) + (p[2] + p[3])) + ((p[4] + p[5]) + (p[6] + p[7])) + (((p[8] + p[9]) + (p[10] + p[11])) + ((p[12] + p[13]) + (p[14] + p[15])));
    {
      int fd = wave >> 1, e = (wave & 1) * 16 + (lane & 15), g = lane >> 4;
      int fe = (e >> 2) & 1, i = ((e >> 3) << 2) | (e & 3);
      *LDS3(u32x2, lds0 + ((fe * 64 + g * 16 + i) * 2 + fd) * 8) = u32x2{pack_bf16(s[0], s[1]), pack_bf16(s[2], s[3])};
      if (tid < 8) *LDS3(u32x2, lds0 + 2048 + ((tid & 3) * 2 + (tid >> 2)) * 8) = u32x2{pack_bf16(zs[0], zs[1]), pack_bf16(zs[2], zs[3])};
    }
    wait_lgkm0(); bar();
    asm volatile("s_waitcnt vmcnt(0)" : "+v"(q[0]), "+v"(q[1]), "+v"(q[2]), "+v"(q[3])::"memory");
    bf16x8 k0 = lds_b128(lds0 + lane * 16), k1 = lds_b128(lds0 + 1024 + lane * 16), zf = lds_b128(lds0 + 2048 + (lane >> 4) * 16);
    f32x4 zero = {0, 0, 0, 0};
#pragma unroll
    for (int i = 0; i < 4; ++i) {
      bf16x8 qq = __builtin_bit_cast(bf16x8, q[i]);
      f32x4 n0 = mfma16(k0, qq, zero), n1 = mfma16(k1, qq, zero), dn = mfma16(zf, qq, zero);
      float inv = 1.f / (dn[0] + 1e-6f);
      u32x4 o = {pack_bf16(n0[0] * inv, n0[1] * inv), pack_bf16(n0[2] * inv, n0[3] * inv), pack_bf16(n1[0] * inv, n1[1] * inv), pack_bf16(n1[2] * inv, n1[3] * inv)};
      st16<(WTM & 2) != 0>(ws + WS_OT, 4 * MB, (((rg * 16 + wave * 4 + i) * 128 + h * 4) * 16 + lane) * 16, o);
    }
  } else {
    int cb = b - 256, l56 = lane < 56 ? lane : 55;
    const char* wp = (const char*)(cb >> 7 ? wu : wg) + (size_t)(((cb >> 3) & 15) * 64 + wave * 16) * FF * 2 + (cb & 7) * 896 + l56 * 16;
    u32x4 v[16];
#pragma unroll
    for (int i = 0; i < 16; ++i) v[i] = *(const u32x4*)(wp + i * FF * 2);
    f32x4 a[2];
#pragma unroll
    for (int c = 0; c < 4; ++c) {
      float lo = 0, hi = 0;
#pragma unroll
      for (int i = 0; i < 16; ++i) { lo += bf_lo(v[i][c]); hi += bf_hi(v[i][c]); }
      a[c >> 1][(c & 1) * 2] = lo; a[c >> 1][(c & 1) * 2 + 1] = hi;
    }
    *LDS3(f32x4, lds0 + wave * 2048 + lane * 32) = a[0];
    *LDS3(f32x4, lds0 + wave * 2048 + lane * 32 + 16) = a[1];
    __syncthreads();
    if (tid < 112) {
      f32x4 s = (*LDS3(f32x4, lds0 + tid * 16) + *LDS3(f32x4, lds0 + 2048 + tid * 16)) + (*LDS3(f32x4, lds0 + 4096 + tid * 16) + *LDS3(f32x4, lds0 + 6144 + tid * 16));
      st16<(WTM & 2) != 0>(ws + WS_CS, 16 * 2 * FF * 4, (((cb >> 3) & 15) * 2 * FF + (cb >> 7) * FF + (cb & 7) * 448 + tid * 4) * 4, __builtin_bit_cast(u32x4, s));
    }
  }
  STAMPW(1, 2);
}

template <int NK> using CR = Cfg<128, 64, 64, 6, 4, 1, 2, NK, 0, 0>;
template <int MODE>
__global__ __launch_bounds__(512, 2) void kR(const void* __restrict__ w, const void* __restrict__ xres, void* __restrict__ out, char* __restrict__ ws) {
  constexpr int K = MODE ? FF : DM;
  using C = CR<K / 64>;
  STAMP(2 + 2 * MODE, 0);
  __shared__ __attribute__((aligned(256))) char sm[C::LDS];
  unsigned lds0 = (unsigned)(size_t)sm;
  int tid = threadIdx.x, lane = tid & 63, wave = __builtin_amdgcn_readfirstlane(tid >> 6);
  int wk = wave & 1, wm = wave >> 1;
  int b = blockIdx.x, xcd = b & 7, idx = b >> 3;
  int tm = (xcd >> 1) * 4 + (idx & 3), tn = (xcd & 1) * 8 + (idx >> 2);
  int g = lane >> 4, r15 = lane & 15;
  int mf = tm * 8 + wm * 2 + wk, row = mf * 16 + r15;
  u32x4 res[2];
#pragma unroll
  for (int jp = 0; jp < 2; ++jp) {
    int c8 = tn * 8 + 2 * (jp * 2 + (g & 1)) + (g >> 1);
    if (MODE == 0) res[jp] = *(const u32x4*)((const char*)xres + ((size_t)row * DM + c8 * 8) * 2);
    else res[jp] = ((const u32x4*)(ws + WS_HT))[(mf * 128 + c8) * 16 + r15];
  }
  Loader<C> L;
  loader_init<C>(L, lds0, wave, lane, ws + (MODE ? WS_ACT : WS_OT), tm * 128, K, w, w, DM, 0, [&](int lc) { return tn * 64 + lc * 8; });
  STAMP3(2, 1);
  prologue<C>(L, wave, lds0);
  STAMP3(2, 2);
  RdAddr<C> R; rd_init<C>(R, lds0, wm, 0, wk, lane, wk, 0, 0);
  f32x4 acc[2][4];
#pragma unroll
  for (int m = 0; m < 2; ++m)
#pragma unroll
    for (int j = 0; j < 4; ++j) acc[m][j] = f32x4{0, 0, 0, 0};
  NoHook hk;
  mainloop<C, 0xfu>(acc, L, R, wave, lds0, hk);
  STAMP(2 + 2 * MODE, 1);
#pragma unroll
  for (int j = 0; j < 4; ++j) *LDS3(f32x4, lds0 + ((wave * 4 + j) * 64 + lane) * 16) = acc[1][j];
  wait_lgkm0(); bar();
#pragma unroll
  for (int j = 0; j < 4; ++j) acc[0][j] += *LDS3(f32x4, lds0 + (((wave ^ 1) * 4 + j) * 64 + lane) * 16);
  float ss = 0, sq = 0;
#pragma unroll
  for (int jp = 0; jp < 2; ++jp) {
    f32x4 a = acc[0][2 * jp], c = acc[0][2 * jp + 1];
#pragma unroll
    for (int r = 0; r < 4; ++r) { float u = a[r], v = c[r]; swap16(u, v); a[r] = u; c[r] = v; }
    u32x4 o;
    o[0] = pack_bf16(a[0] + bf_lo(res[jp][0]), a[1] + bf_hi(res[jp][0]));
    o[1] = pack_bf16(a[2] + bf_lo(res[jp][1]), a[3] + bf_hi(res[jp][1]));
    o[2] = pack_bf16(c[0] + bf_lo(res[jp][2]), c[1] + bf_hi(res[jp][2]));
    o[3] = pack_bf16(c[2] + bf_lo(res[jp][3]), c[3] + bf_hi(res[jp][3]));
    int c8 = tn * 8 + 2 * (jp * 2 + (g & 1)) + (g >> 1);
    if (MODE == 0) {
      st16<(WTM & 4) != 0>(ws + WS_HT, 4 * MB, ((mf * 128 + c8) * 16 + r15) * 16, o);
#pragma unroll
      for (int i = 0; i < 4; ++i) { ss = dot2(o[i], ONES2, ss); sq = dot2(o[i], o[i], sq); }
    } else {
      *(u32x4*)((char*)out + ((size_t)row * DM + c8 * 8) * 2) = o;
    }
  }
  if (MODE == 0) {
    ss = sum_g(ss); sq = sum_g(sq);
    if (g == 0) ((f32x2*)(ws + WS_ST))[row * 16 + tn] = f32x2{ss, sq};
  }
  STAMPW(2 + 2 * MODE, 2);
}

using CD = Cfg<256, 224, 32, 4, 4, 2, 1, 32, 0, 1>;
DEVI float geglu(float x, float u) {
  constexpr float K1 = -2.f * 0.7978845608028654f * 1.4426950408889634f, K2 = K1 * 0.044715f;
  float e = __builtin_amdgcn_exp2f(x * (K1 + K2 * x * x));
  return x * __builtin_amdgcn_rcpf(1.f + e) * u;
}
__global__ __launch_bounds__(512, 2) void kD(const void* __restrict__ wg, const void* __restrict__ wu, const void* __restrict__ wd, char* __restrict__ ws) {
  using C = CD;
  STAMP(3, 0);
  __shared__ __attribute__((aligned(256))) char sm[C::LDS + 4096];
  unsigned lds0 = (unsigned)(size_t)sm;
  constexpr unsigned O_ROW = C::LDS, O_CS = C::LDS + 2048;
  int tid = threadIdx.x, lane = tid & 63, wave = __builtin_amdgcn_readfirstlane(tid >> 6);
  int wn = wave & 1, wm = wave >> 1;
  int b = blockIdx.x, xcd = b & 7, idx = b >> 3;
  int tm = (xcd >> 2) * 4 + (idx & 3), tn = (xcd & 3) * 8 + (idx >> 2);
  {
    Loader<C> L;
    loader_init<C>(L, lds0, wave, lane, ws + WS_HT, tm * 256, DM, wg, wu, FF, 0, [&](int lc) { return tn * 112 + lc * 8; });
    const char* sp = ws + WS_ST + (size_t)(tm * 256 + wave * 32) * 128 + lane * 16;
    f32x4 sv[4];
#pragma unroll
    for (int i = 0; i < 4; ++i) sv[i] = gload4(sp + i * 1024);
    int ct = tid < 448 ? tid >> 1 : 223;
    const float* cp = (const float*)(ws + WS_CS) + (tid & 1) * 16 * FF + (ct < 112 ? tn * 112 + ct : FF + tn * 112 + ct - 112);
    float cv[8];
#pragma unroll
    for (int i = 0; i < 8; ++i) cv[i] = gload1(cp + i * 2 * FF);
    STAMP3(3, 1);
    prologue<C>(L, wave, lds0);
    STAMP3(3, 2);
    wait_stages<C, C::S - 1>(wave);
    asm volatile("" : "+v"(sv[0]), "+v"(sv[1]), "+v"(sv[2]), "+v"(sv[3]), "+v"(cv[0]), "+v"(cv[1]), "+v"(cv[2]), "+v"(cv[3]), "+v"(cv[4]), "+v"(cv[5]), "+v"(cv[6]), "+v"(cv[7]));
    {
      float s[4], q[4];
#pragma unroll
      for (int i = 0; i < 4; ++i) { s[i] = sv[i][0] + sv[i][2]; q[i] = sv[i][1] + sv[i][3]; }
      bool o1 = lane & 1, o2 = lane & 2;
      float sa = (o1 ? s[1] : s[0]) + __shfl_xor(o1 ? s[0] : s[1], 1), sb = (o1 ? s[3] : s[2]) + __shfl_xor(o1 ? s[2] : s[3], 1);
      float qa = (o1 ? q[1] : q[0]) + __shfl_xor(o1 ? q[0] : q[1], 1), qb = (o1 ? q[3] : q[2]) + __shfl_xor(o1 ? q[2] : q[3], 1);
      float ss = (o2 ? sb : sa) + __shfl_xor(o2 ? sa : sb, 2), qq = (o2 ? qb : qa) + __shfl_xor(o2 ? qa : qb, 2);
      ss += __shfl_xor(ss, 4); qq += __shfl_xor(qq, 4);
      float mean = ss * (1.f / DM), var = fmaxf(qq * (1.f / DM) - mean * mean, 0.f);
      float rstd = __builtin_amdgcn_rsqf(var + 1e-5f);
      if (!(lane & 4)) *LDS3(f32x2, lds0 + O_ROW + (wave * 32 + (lane & 3) * 8 + (lane >> 3)) * 8) = f32x2{mean, rstd};
    }
    float cs = ((cv[0] + cv[1]) + (cv[2] + cv[3])) + ((cv[4] + cv[5]) + (cv[6] + cv[7]));
    cs += __shfl_xor(cs, 1);
    if (tid < 448 && !(tid & 1)) *LDS3(float, lds0 + O_CS + ct * 4) = cs;
    RdAddr<C> R; rd_init<C>(R, lds0, wm, wn, 0, lane, 0, 0, 0);
    f32x4 acc[4][7];
#pragma unroll
    for (int m = 0; m < 4; ++m)
#pragma unroll
      for (int j = 0; j < 7; ++j) acc[m][j] = f32x4{0, 0, 0, 0};
    PfHook<1> hk; hk.v = lane * 64; hk.l = lds0 + C::LDS + 3072; hk.p = uni_ptr((const char*)wd + (size_t)b * 28672 + (wave < 7 ? wave : 6) * 4096);
    hk.k1 = wave < 7 ? 4 + 3 * wave : -1;
    mainloop<C, 0x7fu>(acc, L, R, wave, lds0, hk);
    STAMP(3, 1);
    int g = lane >> 4, r15 = lane & 15, hi = g >> 1;
    f32x2 st[2];
#pragma unroll
    for (int p = 0; p < 2; ++p) st[p] = *LDS3(f32x2, lds0 + O_ROW + (wm * 64 + (p * 2 + hi) * 16 + r15) * 8);
    int smf = tm * 16 + wm * 4 + ((g & 1) * 2 + hi);
    u32x4* dst = (u32x4*)(ws + WS_ACT) + ((size_t)smf * (FF / 8) + tn * 14 + wn * 7) * 16 + r15;
    const __amdgpu_buffer_rsrc_t drs = __builtin_amdgcn_make_buffer_rsrc(ws + WS_ACT, 0, SEQ * FF * 2, 0x00020000);
    unsigned doff = ((smf * (FF / 8) + tn * 14 + wn * 7) * 16 + r15) * 16;
#pragma unroll
    for (int j = 0; j < 7; ++j) {
      unsigned ca = lds0 + O_CS + ((wn * 7 + j) * 8 + (g & 1) * 4) * 4;
      f32x4 cg = *LDS3(f32x4, ca), cu = *LDS3(f32x4, ca + 448);
      unsigned pk[2][2];
#pragma unroll
      for (int p = 0; p < 2; ++p) {
        float a[4];
#pragma unroll
        for (int r = 0; r < 4; ++r) {
          float x = acc[2 * p][j][r], y = acc[2 * p + 1][j][r];
          swap32(x, y);
          a[r] = geglu((x - st[p][0] * cg[r]) * st[p][1], (y - st[p][0] * cu[r]) * st[p][1]);
        }
        pk[p][0] = pack_bf16(a[0], a[1]); pk[p][1] = pack_bf16(a[2], a[3]);
      }
      swap16u(pk[0][0], pk[1][0]); swap16u(pk[0][1], pk[1][1]);
      __builtin_amdgcn_raw_buffer_store_b128(u32x4{pk[0][0], pk[0][1], pk[1][0], pk[1][1]}, drs, doff + j * 256, 0, DST);
    }
  }
  STAMPW(3, 2);
}

using CE = Cfg<128, 128, 64, 4, 2, 2, 2, FF / 128, 0, 0>;
typedef __attribute__((address_space(1))) unsigned gu32;
__global__ __launch_bounds__(512, 2) void kE(const void* __restrict__ w, void* __restrict__ out, char* __restrict__ ws) {
  using C = CE;
  STAMP(4, 0);
  __shared__ __attribute__((aligned(256))) char sm[C::LDS];
  unsigned lds0 = (unsigned)(size_t)sm;
  int tid = threadIdx.x, lane = tid & 63, wave = __builtin_amdgcn_readfirstlane(tid >> 6);
  int wk = wave & 1, wn = (wave >> 1) & 1, wm = wave >> 2;
  int b = blockIdx.x, xcd = b & 7, idx = b >> 3;
  int kh = xcd & 1, tm = (xcd >> 1) * 4 + (idx & 3), tn = idx >> 2;
  int g = lane >> 4, r15 = lane & 15;
  int c8 = tn * 16 + wn * 8 + 2 * (2 * wk + (g & 1)) + (g >> 1);
  int mf0 = tm * 8 + wm * 4 + 2 * kh;
  u32x4 res[2];
#pragma unroll
  for (int m = 0; m < 2; ++m) res[m] = ((const u32x4*)(ws + WS_HT))[((mf0 + m) * 128 + c8) * 16 + r15];
  Loader<C> L;
  loader_init<C>(L, lds0, wave, lane, ws + WS_ACT, tm * 128, FF, w, w, DM, kh * (FF / 2), [&](int lc) { return tn * 128 + lc * 8; }, 2 * kh);
  STAMP3(4, 1);
  prologue<C>(L, wave, lds0);
  STAMP3(4, 2);
  RdAddr<C> R; rd_init<C>(R, lds0, wm, wn, wk, lane, 0, 0, 2 * wk);
  f32x4 acc[4][4];
#pragma unroll
  for (int m = 0; m < 4; ++m)
#pragma unroll
    for (int j = 0; j < 4; ++j) acc[m][j] = f32x4{0, 0, 0, 0};
  NoHook hk;
  mainloop<C, 0xfu>(acc, L, R, wave, lds0, hk);
  STAMP(4, 1);
#pragma unroll
  for (int m = 3; m >= 0; --m)
#pragma unroll
    for (int j = 0; j < 2; ++j) *LDS3(f32x4, lds0 + ((wave * 8 + m * 2 + j) * 64 + lane) * 16) = acc[m][2 + j];
  wait_lgkm0(); bar();
  const __amdgpu_buffer_rsrc_t rs = __builtin_amdgcn_make_buffer_rsrc(ws + WS_XCH, 0, 256 * 8 * 4096, 0x00020000);
#pragma unroll
  for (int m = 3; m >= 0; --m)
#pragma unroll
    for (int j = 0; j < 2; ++j) {
      acc[m][j] += *LDS3(f32x4, lds0 + (((wave ^ 1) * 8 + m * 2 + j) * 64 + lane) * 16);
      if (m >= 2) __builtin_amdgcn_raw_buffer_store_b128(__builtin_bit_cast(u32x4, acc[m][j]), rs, (((b * 8 + wave) * 4 + (m - 2) * 2 + j) * 64 + lane) * 16, 0, 16);
    }
  asm volatile("s_waitcnt vmcnt(0)" ::: "memory");
  if (lane == 0) __hip_atomic_store((gu32*)(ws + WS_FLG + (b * 8 + wave) * 128), 1u, __ATOMIC_RELAXED, __HIP_MEMORY_SCOPE_AGENT);
  {
    gu32* pf = (gu32*)(ws + WS_FLG + ((b ^ 1) * 8 + wave) * 128);
    for (int spin = 0; spin < (1 << 22); ++spin) {
      if (__builtin_amdgcn_readfirstlane(__hip_atomic_load(pf, __ATOMIC_RELAXED, __HIP_MEMORY_SCOPE_AGENT)) == 1u) break;
      __builtin_amdgcn_s_sleep(1);
    }
    asm volatile("" ::: "memory");
  }
  u32x4 x[4];
#pragma unroll
  for (int i = 0; i < 4; ++i) x[i] = __builtin_amdgcn_raw_buffer_load_b128(rs, ((((b ^ 1) * 8 + wave) * 4 + i) * 64 + lane) * 16, 0, 16);
#pragma unroll
  for (int m = 0; m < 2; ++m) {
    f32x4 a = acc[m][0] + __builtin_bit_cast(f32x4, x[2 * m]), c = acc[m][1] + __builtin_bit_cast(f32x4, x[2 * m + 1]);
#pragma unroll
    for (int r = 0; r < 4; ++r) { float u = a[r], v = c[r]; swap16(u, v); a[r] = u; c[r] = v; }
    u32x4 o;
    o[0] = pack_bf16(a[0] + bf_lo(res[m][0]), a[1] + bf_hi(res[m][0]));
    o[1] = pack_bf16(a[2] + bf_lo(res[m][1]), a[3] + bf_hi(res[m][1]));
    o[2] = pack_bf16(c[0] + bf_lo(res[m][2]), c[1] + bf_hi(res[m][2]));
    o[3] = pack_bf16(c[2] + bf_lo(res[m][3]), c[3] + bf_hi(res[m][3]));
    st16<(WTM & 8) != 0>(out, 4 * MB, (((mf0 + m) * 16 + r15) * DM + c8 * 8) * 2, o);
  }
  STAMPW(4, 2);
}

extern "C" void kernel_launch(void* const* d_in, const int* in_sizes, int n_in, void* d_out, int out_size, void* d_ws, size_t ws_size,
                              hipStream_t stream) {
  char* ws = (char*)d_ws;
  kA<<<256, 512, 0, stream>>>(d_in[0], d_in[1], d_in[2], d_in[4], d_in[5], d_in[6], ws);
  kB<<<CSA ? 256 : 512, 256, 0, stream>>>(d_in[4], d_in[5], ws);
  kR<0><<<256, 512, 0, stream>>>(d_in[2], d_in[0], nullptr, ws);
  kD<<<256, 512, 0, stream>>>(d_in[4], d_in[5], d_in[6], ws);
  kE<<<256, 512, 0, stream>>>(d_in[6], d_out, ws);
}
```

```cpp
#define ESL0 40
#include <hip/hip_runtime.h>
#include <stdint.h>
#define STAGE_D 0
#define UMAP 1
#define STAG 2

typedef __attribute__((ext_vector_type(4))) short s16x4;
typedef __attribute__((ext_vector_type(8))) short s16x8;
typedef __attribute__((ext_vector_type(8))) __bf16 bf16x8;
typedef __attribute__((ext_vector_type(2))) __bf16 bf16x2;
typedef __attribute__((ext_vector_type(4))) float f32x4;
typedef __attribute__((ext_vector_type(2))) float f32x2;
typedef __attribute__((ext_vector_type(4))) unsigned u32x4;
typedef __attribute__((ext_vector_type(2))) unsigned u32x2;
#define LDS3(T, a) ((__attribute__((address_space(3))) T*)(size_t)(a))
#define DEVI __device__ __forceinline__

template <int N> DEVI void wait_vm() { asm volatile("s_waitcnt vmcnt(%0)" ::"n"(N) : "memory"); }
DEVI void wait_lgkm0() { asm volatile("s_waitcnt lgkmcnt(0)" ::: "memory"); }
DEVI void bar() { __builtin_amdgcn_s_barrier(); }
DEVI void glds16(unsigned voff, const char* sbase, unsigned lds) {
  asm volatile("s_mov_b32 m0, %2\n\ts_nop 0\n\tglobal_load_lds_dwordx4 %0, %1" ::"v"(voff), "s"(sbase), "s"(lds) : "memory");
}
DEVI bf16x8 lds_b128(unsigned a) { return *LDS3(bf16x8, a); }
DEVI s16x4 lds_tr(unsigned a) { return __builtin_amdgcn_ds_read_tr16_b64_v4i16(LDS3(s16x4, a)); }
DEVI unsigned pack_bf16(float lo, float hi) {
  bf16x2 v; v[0] = (__bf16)lo; v[1] = (__bf16)hi; return __builtin_bit_cast(unsigned, v);
}
DEVI float bf_lo(unsigned u) { return __uint_as_float(u << 16); }
DEVI float bf_hi(unsigned u) { return __uint_as_float(u & 0xffff0000u); }
DEVI float dot2(unsigned a, unsigned b, float c) {
  return __builtin_amdgcn_fdot2_f32_bf16(__builtin_bit_cast(bf16x2, a), __builtin_bit_cast(bf16x2, b), c, false);
}
DEVI void swap16(float& a, float& b) {
  auto r = __builtin_amdgcn_permlane16_swap(__float_as_uint(a), __float_as_uint(b), false, false);
  a = __uint_as_float(r[0]); b = __uint_as_float(r[1]);
}
DEVI void swap16u(unsigned& a, unsigned& b) {
  auto r = __builtin_amdgcn_permlane16_swap(a, b, false, false);
  a = r[0]; b = r[1];
}
DEVI void swap32(float& a, float& b) {
  auto r = __builtin_amdgcn_permlane32_swap(__float_as_uint(a), __float_as_uint(b), false, false);
  a = __uint_as_float(r[0]); b = __uint_as_float(r[1]);
}
DEVI float sum_g(float x) {
  float a = x, b = x; swap16(a, b); x = a + b;
  a = x; b = x; swap32(a, b); return a + b;
}
DEVI f32x4 mfma16(bf16x8 a, bf16x8 b, f32x4 c) { return __builtin_amdgcn_mfma_f32_16x16x32_bf16(a, b, c, 0, 0, 0); }

template <int BM_, int BN_, int BK_, int S_, int WM_, int WN_, int WK_, int NK_, int AMODE_, int BMODE_, int NPF_ = 0>
struct Cfg {
  static constexpr int NPF = NPF_;
  static constexpr int BM = BM_, BN = BN_, BK = BK_, S = S_, WM = WM_, WN = WN_, WK = WK_, NK = NK_, AMODE = AMODE_, BMODE = BMODE_;
  static constexpr int NW = WM * WN * WK;
  static constexpr int MF = BM / WM / 16, NF = BN / WN / 16, KF = BK / 32, KFW = KF / WK;
  static constexpr int A_BYTES = BM * BK * 2;
  static constexpr int B_BYTES = BN * BK * 2 + (BMODE == 1 ? 256 : 0);
  static constexpr int STAGE = A_BYTES + B_BYTES;
  static constexpr int PA = A_BYTES / 1024, PB = BN * BK * 2 / 1024, P = PA + PB;
  static constexpr int NLW = NPF ? NW - 1 : NW;
  static constexpr int NP = (P + NLW - 1) / NLW;
  static constexpr int FULLW = P - (NP - 1) * NLW;
  static constexpr int RS = (BMODE == 1 ? BN : BN * 2);
  static constexpr int LDS = S * STAGE;
  static constexpr bool SLOT = (MF == 2);
};

template <class C> struct Frags { bf16x8 a[C::KFW][C::MF]; bf16x8 b[C::KFW][C::NF]; };
template <class C> struct Loader {
  const char* sbase[C::NP]; unsigned voff[C::NP]; unsigned ldso[C::NP]; unsigned sstr[C::NP];
  const char* pfb[4]; unsigned pfv, pfl, pfs[4];
};
DEVI void glds4(unsigned voff, const char* sbase, unsigned lds) {
  asm volatile("s_mov_b32 m0, %2\n\ts_nop 0\n\tglobal_load_lds_dword %0, %1" ::"v"(voff), "s"(sbase), "s"(lds) : "memory");
}
template <class C> DEVI void issue_pf(Loader<C>& L, int i) { glds4(L.pfv, L.pfb[i], L.pfl); L.pfb[i] += L.pfs[i]; }
DEVI const char* uni_ptr(const char* p) {
  unsigned long long u = (unsigned long long)p;
  unsigned lo = __builtin_amdgcn_readfirstlane((unsigned)u), hi = __builtin_amdgcn_readfirstlane((unsigned)(u >> 32));
  return (const char*)(((unsigned long long)hi << 32) | lo);
}

template <class C> DEVI void issue_piece(Loader<C>& L, int i, int wave, unsigned bufbase) {
  if (i < C::NP - 1 ? (C::NLW == C::NW || wave < C::NLW) : (C::FULLW == C::NW || wave < C::FULLW)) glds16(L.voff[i], L.sbase[i], bufbase + L.ldso[i]);
  L.sbase[i] += L.sstr[i];
}
template <class C, class ColMap>
DEVI void loader_init(Loader<C>& L, unsigned lds0, int wave, int lane, const void* A, int m0, int lda, const void* B0, const void* B1, int ldb, int k0,
                      ColMap colmap, int mxor = 0) {
#pragma unroll
  for (int i = 0; i < C::NP; ++i) {
    int p = wave + i * C::NLW;
    if (p >= C::P) p = C::P - 1;
    if (p < C::PA) {
      if (C::AMODE == 0) {
        int mb = p / C::KF, kb = p % C::KF;
        L.sbase[i] = (const char*)A + ((size_t)(m0 / 16 + (mb ^ mxor)) * (lda / 8) + (k0 / 8 + kb * 4)) * 256;
        L.voff[i] = lane * 16;
        L.sstr[i] = C::BK / 8 * 256;
      } else {
        int row = 8 * p + (lane >> 3), pos = lane & 7, lc = pos ^ ((row >> 1) & 7);
        L.sbase[i] = (const char*)A + ((size_t)(m0 + 8 * p) * lda + k0) * 2;
        L.voff[i] = ((lane >> 3) * lda + lc * 8) * 2;
        L.sstr[i] = C::BK * 2;
      }
      L.ldso[i] = p * 1024;
    } else {
      int pb = p - C::PA;
      if (C::BMODE == 0) {
        constexpr int CH = C::BN / 8;
        int ci = pb * 64 + lane, k = ci / CH, c = ci % CH, u = c >> 1;
        int sg = (C::RS % 256 == 0) ? ((k & 3) | (((k >> 3) & 1) << 2)) : (C::RS % 256 == 128) ? (((k >> 1) & 1) | (((k >> 3) & 1) << 1)) : ((k >> 3) & 1);
        int lc = ((u ^ sg) << 1) | (c & 1);
        L.sbase[i] = (const char*)B0 + (size_t)k0 * ldb * 2;
        L.voff[i] = (k * ldb + colmap(lc)) * 2;
        L.ldso[i] = C::A_BYTES + pb * 1024;
      } else {
        constexpr int CH = C::BN / 16, HP = C::PB / 2;
        int half = pb / HP, pp = pb % HP;
        int ci = pp * 64 + lane, k = ci / CH, c = ci % CH, lc = c ^ ((k >> 3) & 1);
        L.sbase[i] = (const char*)(half ? B1 : B0) + (size_t)k0 * ldb * 2;
        L.voff[i] = (k * ldb + colmap(lc)) * 2;
        L.ldso[i] = C::A_BYTES + half * (HP * 1024 + 128) + pp * 1024;
      }
      L.sstr[i] = C::BK * ldb * 2;
    }
    {
      unsigned long long u = (unsigned long long)L.sbase[i];
      unsigned lo = __builtin_amdgcn_readfirstlane((unsigned)u), hi = __builtin_amdgcn_readfirstlane((unsigned)(u >> 32));
      L.sbase[i] = (const char*)(((unsigned long long)hi << 32) | lo);
      L.ldso[i] = __builtin_amdgcn_readfirstlane(L.ldso[i]);
      L.sstr[i] = __builtin_amdgcn_readfirstlane(L.sstr[i]);
    }
    issue_piece<C>(L, i, wave, lds0);
  }
}

template <class C, int NST, int X = 0> DEVI void wait_stages(int wave) {
  if (C::FULLW == C::NW || wave < C::FULLW) wait_vm<NST * C::NP + X>();
  else if (C::NLW == C::NW || wave < C::NLW) wait_vm<NST*(C::NP - 1) + X>();
}

template <class C> struct RdAddr { unsigned a[C::KFW][2]; unsigned b[C::NF]; unsigned kbo[2]; };
template <class C> DEVI void rd_init(RdAddr<C>& R, unsigned lds0, int wm, int wn, int wk, int lane, int mx, int kx, int jx) {
  int g = lane >> 4, r15 = lane & 15, q = (lane & 15) >> 2, p = lane & 3;
#pragma unroll
  for (int kb = 0; kb < C::KFW; ++kb)
#pragma unroll
    for (int m = 0; m < 2; ++m) {
      int ka = wk * C::KFW + (kb ^ kx), ma = wm * C::MF + (m ^ mx);
      if (C::AMODE == 0) R.a[kb][m] = lds0 + (ma * C::KF + ka) * 1024 + lane * 16;
      else R.a[kb][m] = lds0 + (ma * 16 + r15) * 128 + (((ka * 4 + g) ^ ((r15 >> 1) & 7)) * 16);
    }
  R.kbo[0] = kx * 32 * C::RS; R.kbo[1] = (1 ^ kx) * 32 * C::RS;
  int krow = wk * C::KFW * 32 + 8 * g + q;
#pragma unroll
  for (int j = 0; j < C::NF; ++j) {
    int U = wn * C::NF + (j ^ jx);
    if (UMAP && C::AMODE == 1) U = (j >> 1) * 4 + wn * 2 + ((j ^ jx) & 1);
    if (C::BMODE == 0) {
      int sg = (C::RS % 256 == 0) ? (q | ((g & 1) << 2)) : (C::RS % 256 == 128) ? ((q >> 1) | ((g & 1) << 1)) : (g & 1);
      R.b[j] = lds0 + C::A_BYTES + krow * C::RS + ((U ^ sg) * 32) + p * 8;
    } else {
      R.b[j] = lds0 + C::A_BYTES + (p >> 1) * (C::PB / 2 * 1024 + 128) + krow * C::RS + ((U ^ (g & 1)) * 16) + (p & 1) * 8;
    }
  }
}
template <class C> DEVI void read_a(Frags<C>& f, const RdAddr<C>& R, unsigned so, int kb, int mf) {
  if (C::SLOT) f.a[kb][mf] = lds_b128(R.a[kb][mf & 1] + so);
  else f.a[kb][mf] = lds_b128(R.a[0][0] + so + (mf * C::KF + kb) * 1024);
}
template <class C> DEVI void read_b(Frags<C>& f, const RdAddr<C>& R, unsigned so, int kb, int j) {
  unsigned a = (C::AMODE == 1) ? R.b[j] + (so + R.kbo[kb & 1]) : R.b[j] + so + (kb * 32) * C::RS;
  s16x4 lo = lds_tr(a);
  s16x4 hi = lds_tr(a + 4 * C::RS);
  s16x8 v = __builtin_shufflevector(lo, hi, 0, 1, 2, 3, 4, 5, 6, 7);
  f.b[kb][j] = __builtin_bit_cast(bf16x8, v);
}
template <class C> DEVI void read_all(Frags<C>& f, const RdAddr<C>& R, unsigned so) {
#pragma unroll
  for (int kb = 0; kb < C::KFW; ++kb) {
#pragma unroll
    for (int m = 0; m < C::MF; ++m) read_a<C>(f, R, so, kb, m);
#pragma unroll
    for (int j = 0; j < C::NF; ++j) read_b<C>(f, R, so, kb, j);
  }
}

template <class C, unsigned SWAPMASK, bool ISSUE, bool READ, int NOUT, int PF, class Hook>
DEVI void iter(f32x4 (&acc)[C::MF][C::NF], const Frags<C>& cur, Frags<C>& nxt, Loader<C>& L, const RdAddr<C>& R, int wave, unsigned lds0,
               unsigned& bcur, Hook& hook, int kt) {
  wait_lgkm0();
  if (READ) { wait_stages<C, NOUT, (PF == 10 ? Hook::NT : 0)>(wave); }
  bar();
  if (PF == 9) hook.tail();
  unsigned bn = bcur + C::STAGE; if (bn == C::LDS) bn = 0;
  constexpr int NM = C::KFW * C::MF * C::NF;
  constexpr int NR = C::KFW * (C::MF + C::NF);
  constexpr int SEG = C::NP + 1;
#pragma unroll
  for (int s = 0; s < SEG; ++s) {
    if (READ) {
#pragma unroll
      for (int r = s * NR / SEG; r < (s + 1) * NR / SEG; ++r) {
        int kb = r / (C::MF + C::NF), x = r % (C::MF + C::NF);
        if (x < C::MF) read_a<C>(nxt, R, bn, kb, x); else read_b<C>(nxt, R, bn, kb, x - C::MF);
      }
    }
#pragma unroll
    for (int i = s * NM / SEG; i < (s + 1) * NM / SEG; ++i) {
      int kb = i / (C::MF * C::NF), x = i % (C::MF * C::NF), j = x / C::MF, m = x % C::MF;
      if ((SWAPMASK >> j) & 1) acc[m][j] = mfma16(cur.b[kb][j], cur.a[kb][m], acc[m][j]);
      else acc[m][j] = mfma16(cur.a[kb][m], cur.b[kb][j], acc[m][j]);
    }
    hook.seg(cur, s, SEG);
#pragma unroll
    for (int x = 0; x < 8; ++x) {
      __builtin_amdgcn_sched_group_barrier(0x8, 1, 0);
      __builtin_amdgcn_sched_group_barrier(0x100, 1, 0);
      if (Hook::VALU) __builtin_amdgcn_sched_group_barrier(0x2, Hook::VALU, 0);
    }
    __builtin_amdgcn_sched_barrier(0);
    if (PF && PF < 9 && (s == 1 || s == 3) && (PF - 1) * 2 + (s >> 1) < C::NPF) { if (wave == C::NW - 1) issue_pf<C>(L, (PF - 1) * 2 + (s >> 1)); }
    if (ISSUE && s < C::NP) { issue_piece<C>(L, s, wave, lds0 + bcur); __builtin_amdgcn_sched_barrier(0); }
    if (s == SEG - 1) hook.mid(kt);
  }
  bcur = bn;
}

template <class C> DEVI void prologue(Loader<C>& L, int wave, unsigned lds0) {
#pragma unroll
  for (int s = 1; s < C::S; ++s) {
#pragma unroll
    for (int i = 0; i < C::NP; ++i) issue_piece<C>(L, i, wave, lds0 + s * C::STAGE);
  }
}

template <class C, unsigned SWAPMASK, class Hook>
DEVI void mainloop(f32x4 (&acc)[C::MF][C::NF], Loader<C>& L, const RdAddr<C>& R, int wave, unsigned lds0, Hook& hook) {
  constexpr int S = C::S, NK = C::NK;
  Frags<C> f0, f1;
  wait_stages<C, S - 1>(wave);
  bar();
  read_all<C>(f0, R, 0);
  unsigned bcur = 0;
  constexpr int NMAIN = NK - S;
  constexpr int NPAIR = NMAIN / 2;
  for (int it = 0; it < NPAIR; ++it) {
    iter<C, SWAPMASK, true, true, S - 2, (C::NPF > 0 ? 1 : 0)>(acc, f0, f1, L, R, wave, lds0, bcur, hook, 2 * it);
    iter<C, SWAPMASK, true, true, S - 2, (C::NPF > 2 ? 2 : 0)>(acc, f1, f0, L, R, wave, lds0, bcur, hook, 2 * it + 1);
  }
  constexpr int T0 = 2 * NPAIR;
#define TAILIT(T, CUR, NXT)                                                        \
  if constexpr (T0 + (T) < NK) {                                                   \
    constexpr int kt = T0 + (T);                                                   \
    constexpr bool iss = kt + S < NK, rd = kt + 1 < NK;                            \
    constexpr int last_issued = (kt + S - 1 < NK - 1) ? kt + S - 1 : NK - 1;       \
    constexpr int nout = rd ? last_issued - (kt + 1) : 0;                          \
    iter<C, SWAPMASK, iss, rd, nout, (kt == NK - S ? 9 : kt > NK - S ? 10 : 0)>(acc, CUR, NXT, L, R, wave, lds0, bcur, hook, kt); \
  }
  TAILIT(0, f0, f1) TAILIT(1, f1, f0) TAILIT(2, f0, f1) TAILIT(3, f1, f0) TAILIT(4, f0, f1) TAILIT(5, f1, f0) TAILIT(6, f0, f1) TAILIT(7, f1, f0)
#undef TAILIT
}
struct NoHook { static constexpr int VALU = 0, NT = 0; template <class F> DEVI void seg(const F&, int, int) const {} DEVI void tail() const {} DEVI void mid(int) const {} };
template <int N> struct PfHook {
  static constexpr int VALU = 0, NT = (STAGE_D ? 0 : N);
  const char* p; unsigned v, l; int k1;
  template <class F> DEVI void seg(const F&, int, int) const {}
  DEVI void tail() const {
#pragma unroll
    for (int i = 0; i < N; ++i) glds4(v, p + i * 4096, l);
  }
  DEVI void mid(int kt) const {
  }
};

#define FILL 1
#define ESL 1
#define FST 4
#define FWT 1
#define TNX 0
#define LINPF 2
#define DST 16
#define CSA 1
#define STAGD 4
#define STAG0 3
constexpr int SEQ = 2048, DM = 1024, FF = 3584;
constexpr size_t MB = 1u << 20;
constexpr size_t WS_QF = 0;
constexpr size_t WS_OT = 4 * MB;
constexpr size_t WS_HT = 8 * MB;
constexpr size_t WS_KVP = 12 * MB;
constexpr size_t WS_ZP = 14 * MB;
constexpr size_t WS_ST = 14 * MB + 256 * 1024;
constexpr size_t WS_CS = 14 * MB + 768 * 1024;
constexpr size_t WS_ACT = 16 * MB;
constexpr size_t WS_XCH = 32 * MB;
#define DIAG3 0
#define STAMP3(kid, pos)
#define STAMP(kid, i)
#define STAMPW(kid, i)
constexpr unsigned ONES2 = 0x3f803f80u;
#define WTM 15
template <bool WT> DEVI void st16(void* base, unsigned bytes, unsigned off, u32x4 v) {
  if (WT) __builtin_amdgcn_raw_buffer_store_b128(v, __builtin_amdgcn_make_buffer_rsrc(base, 0, bytes, 0x00020000), off, 0, 16);
  else *(u32x4*)((char*)base + off) = v;
}

DEVI f32x4 gload4(const void* p) { f32x4 r; asm volatile("global_load_dwordx4 %0, %1, off" : "=v"(r) : "v"(p) : "memory"); return r; }
DEVI float gload1(const void* p) { float r; asm volatile("global_load_dword %0, %1, off" : "=v"(r) : "v"(p) : "memory"); return r; }
DEVI float phi(float t) { return (t > 0.f ? t + 1.f : __expf(t)) + 1e-6f; }

#define SV 0
#define SVV 1
#define SA 3
using CA = Cfg<128, 192, 64, SA, 4, 2, 1, 16, 1, 0, 0>;
DEVI void touch(unsigned& d, unsigned voff, const char* sbase, int a, int b) {
  asm volatile("s_cmp_lg_u32 %3, %4\n\ts_cbranch_scc1 .Ltch%=\n\tglobal_load_dword %0, %1, %2\n.Ltch%=:" : "+v"(d) : "v"(voff), "s"(sbase), "s"(a), "s"(b) : "memory", "scc");
}
struct HookA {
  static constexpr int VALU = SV ? SVV : 0, NT = 1;
  f32x4 rs, rq, c[3];
  const char* p; unsigned v, v2, d; int k1;
  char* xch; unsigned xo;
  DEVI void fill() const {
#pragma unroll
    for (int i = 0; i < 4; ++i) st16<FWT>(xch, 8 * MB, xo + i * 1024, u32x4{~0u, ~0u, ~0u, ~0u});
  }
  DEVI void tail() { touch(d, v2, p, k1, k1); if (FILL == 5) fill(); }
  DEVI void mid(int kt) {
    if (kt < CA::NK - CA::S) touch(d, v, p, kt, k1);
  }
  template <bool V> DEVI void acc1(f32x4& r, bf16x8 x, bool sqr) {
    bf16x8 ones = __builtin_bit_cast(bf16x8, u32x4{ONES2, ONES2, ONES2, ONES2});
    if (V) {
      u32x4 u = __builtin_bit_cast(u32x4, x);
#pragma unroll
      for (int i = 0; i < 4; ++i) r[i & 1] = dot2(u[i], sqr ? u[i] : ONES2, r[i & 1]);
    } else r = mfma16(sqr ? x : ones, x, r);
  }
  template <class F> DEVI void seg(const F& f, int s, int SEG) {
#pragma unroll
    for (int u = s * 7 / SEG; u < (s + 1) * 7 / SEG; ++u) {
      if (u == 0) acc1<(SV & 1) != 0>(rs, f.a[0][0], false);
      if (u == 1) acc1<(SV & 2) != 0>(rq, f.a[0][0], true);
      if (u == 2) acc1<(SV & 4) != 0>(c[0], f.b[0][0], false);
      if (u == 3) acc1<(SV & 4) != 0>(c[1], f.b[0][2], false);
      if (u == 4) acc1<(SV & 4) != 0>(c[2], f.b[0][4], false);
      if (u == 5) acc1<(SV & 1) != 0>(rs, f.a[1][0], false);
      if (u == 6) acc1<(SV & 2) != 0>(rq, f.a[1][0], true);
    }
  }
};

__global__ __launch_bounds__(512, 2) void kA(const void* __restrict__ x, const void* __restrict__ wqkv, const void* __restrict__ wo, const void* __restrict__ wg,
                                             const void* __restrict__ wu, const void* __restrict__ wd, char* __restrict__ ws) {
  using C = CA;
  STAMP(0, 0);
  __shared__ __attribute__((aligned(256))) char sm[C::LDS];
  unsigned lds0 = (unsigned)(size_t)sm;
  int tid = threadIdx.x, lane = tid & 63, wave = __builtin_amdgcn_readfirstlane(tid >> 6);
  int wn = wave & 1, wm = wave >> 1;
  int b = blockIdx.x, xcd = b & 7, idx = b >> 3;
  int tm = (xcd >> 1) * 4 + (idx & 3), tn = (xcd & 1) * 8 + ((idx >> 2) ^ TNX);
  Loader<C> L;
  loader_init<C>(L, lds0, wave, lane, x, tm * 128, DM, wqkv, wqkv, 3 * DM, 0, [&](int lc) {
    return (lc >> 3) * DM + tn * 64 + (lc & 7) * 8;
  });
  STAMP3(0, 1);
  prologue<C>(L, wave, lds0);
  STAMP3(0, 2);
  HookA hk; hk.d = 0; hk.xch = ws + WS_XCH; hk.xo = ((b * 8 + wave) * 256 + lane) * 16;
  touch(hk.d, lane * 64, uni_ptr((const char*)wqkv + (size_t)b * 24576 + (wave < 6 ? wave : 0) * 4096), (wave + 2) >> 3, 0);
  int mx = wn, kx = wm & 1, jx = wm >> 1;
  RdAddr<C> R; rd_init<C>(R, lds0, wm, wn, 0, lane, mx, kx, jx);
  f32x4 acc[2][6];
#pragma unroll
  for (int m = 0; m < 2; ++m)
#pragma unroll
    for (int j = 0; j < 6; ++j) acc[m][j] = f32x4{0, 0, 0, 0};
  const char* wp = uni_ptr((const char*)(b >> 7 ? wu : wg) + (size_t)((b >> 3) & 15) * 64 * FF * 2 + (b & 7) * 896);
  {
    int i1 = wave * 128 + lane, i2 = i1 + 64;
    hk.p = wave < 7 ? wp : uni_ptr((const char*)wo + (size_t)b * 8192);
    hk.v = wave < 7 ? (i1 / 14) * (FF * 2) + (i1 % 14) * 64 : lane * 64;
    hk.v2 = wave < 7 ? (i2 / 14) * (FF * 2) + (i2 % 14) * 64 : lane * 64 + 4096;
    hk.k1 = STAG0 + wave;
  }
  hk.rs = hk.rq = hk.c[0] = hk.c[1] = hk.c[2] = f32x4{0, 0, 0, 0};
  if (FILL == 7) hk.fill();
  mainloop<C, 0x3u>(acc, L, R, wave, lds0, hk);
  STAMP(0, 1);
  if (FILL == 6) hk.fill();
  u32x4 wv[8];
  if (CSA) {
    int l56 = lane < 56 ? lane : 55;
#pragma unroll
    for (int i = 0; i < 8; ++i) wv[i] = *(const __attribute__((address_space(1))) u32x4*)(size_t)(wp + (wave * 8 + i) * (FF * 2) + l56 * 16);
  }
  int g = lane >> 4, r15 = lane & 15;
  constexpr unsigned O_ROW = 0, O_CS = 1024, O_Z = 4096, O_RED = 8192, O_WP = 40960;
  {
    int rr = r15 & 3;
    float s = hk.rs[0], q = rr == 0 ? hk.rq[0] : rr == 1 ? hk.rq[1] : rr == 2 ? hk.rq[2] : hk.rq[3];
    if (SV & 1) s = sum_g(hk.rs[0] + hk.rs[1]);
    if (SV & 2) q = sum_g(hk.rq[0] + hk.rq[1]);
    if (SV & 4) {
#pragma unroll
      for (int i = 0; i < 3; ++i) hk.c[i][0] = sum_g(hk.c[i][0] + hk.c[i][1]);
    }
    float mean = s * (1.f / DM), var = fmaxf(q * (1.f / DM) - mean * mean, 0.f);
    float rstd = __builtin_amdgcn_rsqf(var + 1e-5f);
    if (g == (r15 >> 2)) *LDS3(f32x2, lds0 + O_ROW + (wm * 32 + wn * 16 + r15) * 8) = f32x2{mean, rstd};
    float cc = g == 1 ? hk.c[0][0] : g == 2 ? hk.c[1][0] : hk.c[2][0];
    if (g) *LDS3(float, lds0 + O_CS + (kx * 192 + wn * 96 + (((g - 1) * 2) ^ jx) * 16 + r15) * 4) = cc;
  }
  wait_lgkm0(); bar();
  int head = 2 * tn + wn;
#pragma unroll
  for (int m = 0; m < 2; ++m) {
    int ma = m ^ mx;
    f32x2 st = *LDS3(f32x2, lds0 + O_ROW + (wm * 32 + ma * 16 + r15) * 8);
    unsigned pk[2][2];
#pragma unroll
    for (int j = 0; j < 2; ++j) {
      unsigned ca = lds0 + O_CS + (wn * 96 + (j ^ jx) * 16 + 4 * g) * 4;
      f32x4 cs = *LDS3(f32x4, ca) + *LDS3(f32x4, ca + 768);
      float v[4];
#pragma unroll
      for (int r = 0; r < 4; ++r) v[r] = phi((acc[m][j][r] - st[0] * cs[r]) * st[1]);
      pk[j][0] = pack_bf16(v[0], v[1]); pk[j][1] = pack_bf16(v[2], v[3]);
    }
    u32x4 o = jx ? u32x4{pk[1][0], pk[1][1], pk[0][0], pk[0][1]} : u32x4{pk[0][0], pk[0][1], pk[1][0], pk[1][1]};
    st16<WTM & 1>(ws + WS_QF, 4 * MB, (((tm * 8 + wm * 2 + ma) * 32 + head) * 64 + lane) * 16, o);
  }
  {
    f32x4 sa[2][2];
#pragma unroll
    for (int m = 0; m < 2; ++m) {
      unsigned ra = lds0 + O_ROW + (wm * 32 + (m ^ mx) * 16 + 4 * g) * 8;
      sa[m][0] = *LDS3(f32x4, ra); sa[m][1] = *LDS3(f32x4, ra + 16);
    }
    bf16x8 fr[4];
#pragma unroll
    for (int j = 2; j < 6; ++j) {
      unsigned ca = lds0 + O_CS + (wn * 96 + (j ^ jx) * 16 + r15) * 4;
      float cs = *LDS3(float, ca) + *LDS3(float, ca + 768);
      u32x4 u;
#pragma unroll
      for (int m = 0; m < 2; ++m) {
        float v[4];
#pragma unroll
        for (int r = 0; r < 4; ++r) {
          float t = (acc[m][j][r] - sa[m][r >> 1][(r & 1) * 2] * cs) * sa[m][r >> 1][(r & 1) * 2 + 1];
          v[r] = j < 4 ? phi(t) : t;
        }
        u[m * 2] = pack_bf16(v[0], v[1]); u[m * 2 + 1] = pack_bf16(v[2], v[3]);
      }
      fr[j - 2] = __builtin_bit_cast(bf16x8, u);
    }
    bf16x8 ones = __builtin_bit_cast(bf16x8, u32x4{ONES2, ONES2, ONES2, ONES2});
    f32x4 zero = {0, 0, 0, 0};
#pragma unroll
    for (int fd = 0; fd < 2; ++fd) {
#pragma unroll
      for (int fe = 0; fe < 2; ++fe) {
        f32x4 kv = mfma16(fr[fd], fr[2 + fe], zero);
        *LDS3(f32x4, lds0 + O_RED + ((wave * 4 + ((fd ^ jx) * 2 + (fe ^ jx))) * 64 + lane) * 16) = kv;
      }
      f32x4 z = mfma16(fr[fd], ones, zero);
      if (r15 == 0) *LDS3(f32x4, lds0 + O_Z + ((wave * 2 + (fd ^ jx)) * 4 + g) * 16) = z;
    }
  }
  if (CSA) {
    f32x4 a[2];
#pragma unroll
    for (int c = 0; c < 4; ++c) {
      float lo = 0, hi = 0;
#pragma unroll
      for (int i = 0; i < 8; ++i) { lo += bf_lo(wv[i][c]); hi += bf_hi(wv[i][c]); }
      a[c >> 1][(c & 1) * 2] = lo; a[c >> 1][(c & 1) * 2 + 1] = hi;
    }
    *LDS3(f32x4, lds0 + O_WP + wave * 2048 + lane * 32) = a[0];
    *LDS3(f32x4, lds0 + O_WP + wave * 2048 + lane * 32 + 16) = a[1];
    asm volatile("" ::"v"(hk.d), "v"(a[0]), "v"(a[1]));
  }
  wait_lgkm0(); bar();
  if (CSA && tid >= 384 && tid < 496) {
    int t = tid - 384;
    f32x4 s = *LDS3(f32x4, lds0 + O_WP + t * 16);
#pragma unroll
    for (int k = 1; k < 8; ++k) s += *LDS3(f32x4, lds0 + O_WP + k * 2048 + t * 16);
    st16<WTM & 1>(ws + WS_CS, 16 * 2 * FF * 4, (((b >> 3) & 15) * 2 * FF + (b >> 7) * FF + (b & 7) * 448 + t * 4) * 4, __builtin_bit_cast(u32x4, s));
  }
  {
    int w2 = tid >> 8, i = tid & 255;
    f32x4 s = *LDS3(f32x4, lds0 + O_RED + (w2 * 256 + i) * 16);
#pragma unroll
    for (int k = 1; k < 4; ++k) s += *LDS3(f32x4, lds0 + O_RED + ((k * 2 + w2) * 256 + i) * 16);
    st16<WTM & 1>(ws + WS_KVP, 2 * MB, (((2 * tn + w2) * 16 + tm) * 256 + i) * 16, __builtin_bit_cast(u32x4, s));
    if (tid < 16) {
      int w3 = tid >> 3, c = tid & 7;
      f32x4 z = *LDS3(f32x4, lds0 + O_Z + (w3 * 8 + c) * 16);
#pragma unroll
      for (int k = 1; k < 4; ++k) z += *LDS3(f32x4, lds0 + O_Z + ((k * 2 + w3) * 8 + c) * 16);
      ((f32x4*)(ws + WS_ZP))[((2 * tn + w3) * 16 + tm) * 8 + c] = z;
    }
  }
  STAMPW(0, 2);
}

__global__ __launch_bounds__(256) void kB(const void* __restrict__ wg, const void* __restrict__ wu, char* __restrict__ ws) {
  __shared__ __attribute__((aligned(16))) char sm[8192];
  unsigned lds0 = (unsigned)(size_t)sm;
  int tid = threadIdx.x, lane = tid & 63, wave = tid >> 6, b = blockIdx.x;
  STAMP(1, 0); STAMP(1, 1);
  if (CSA || b < 256) {
    int h = (b & 7) * 4 + ((b >> 3) & 3), rg = b >> 5;
    const f32x4* kvp = (const f32x4*)(ws + WS_KVP) + h * 4096 + tid;
    const f32x4* zp = (const f32x4*)(ws + WS_ZP) + h * 128 + lane;
    f32x4 z0 = gload4(zp), z1 = gload4(zp + 64);
    f32x4 p[16];
#pragma unroll
    for (int i = 0; i < 16; ++i) p[i] = gload4(kvp + i * 256);
    u32x4 q[4];
#pragma unroll
    for (int i = 0; i < 4; ++i) q[i] = __builtin_bit_cast(u32x4, gload4((const u32x4*)(ws + WS_QF) + ((rg * 16 + wave * 4 + i) * 32 + h) * 64 + lane));
    asm volatile("s_waitcnt vmcnt(4)" : "+v"(z0), "+v"(z1), "+v"(p[0]), "+v"(p[1]), "+v"(p[2]), "+v"(p[3]), "+v"(p[4]), "+v"(p[5]), "+v"(p[6]), "+v"(p[7])::"memory");
    asm volatile("" : "+v"(p[8]), "+v"(p[9]), "+v"(p[10]), "+v"(p[11]), "+v"(p[12]), "+v"(p[13]), "+v"(p[14]), "+v"(p[15])::"memory");
    f32x4 zs = z0 + z1;
#pragma unroll
    for (int c = 0; c < 4; ++c) { zs[c] = sum_g(zs[c]); zs[c] += __shfl_xor(zs[c], 8); }
    f32x4 s = ((p[0] + p[1]) + (p[2] + p[3])) + ((p[4] + p[5]) + (p[6] + p[7])) + (((p[8] + p[9]) + (p[10] + p[11])) + ((p[12] + p[13]) + (p[14] + p[15])));
    {
      int fd = wave >> 1, e = (wave & 1) * 16 + (lane & 15), g = lane >> 4;
      int fe = (e >> 2) & 1, i = ((e >> 3) << 2) | (e & 3);
      *LDS3(u32x2, lds0 + ((fe * 64 + g * 16 + i) * 2 + fd) * 8) = u32x2{pack_bf16(s[0], s[1]), pack_bf16(s[2], s[3])};
      if (tid < 8) *LDS3(u32x2, lds0 + 2048 + ((tid & 3) * 2 + (tid >> 2)) * 8) = u32x2{pack_bf16(zs[0], zs[1]), pack_bf16(zs[2], zs[3])};
    }
    wait_lgkm0(); bar();
    asm volatile("s_waitcnt vmcnt(0)" : "+v"(q[0]), "+v"(q[1]), "+v"(q[2]), "+v"(q[3])::"memory");
    bf16x8 k0 = lds_b128(lds0 + lane * 16), k1 = lds_b128(lds0 + 1024 + lane * 16), zf = lds_b128(lds0 + 2048 + (lane >> 4) * 16);
    f32x4 zero = {0, 0, 0, 0};
#pragma unroll
    for (int i = 0; i < 4; ++i) {
      bf16x8 qq = __builtin_bit_cast(bf16x8, q[i]);
      f32x4 n0 = mfma16(k0, qq, zero), n1 = mfma16(k1, qq, zero), dn = mfma16(zf, qq, zero);
      float inv = 1.f / (dn[0] + 1e-6f);
      u32x4 o = {pack_bf16(n0[0] * inv, n0[1] * inv), pack_bf16(n0[2] * inv, n0[3] * inv), pack_bf16(n1[0] * inv, n1[1] * inv), pack_bf16(n1[2] * inv, n1[3] * inv)};
      st16<(WTM & 2) != 0>(ws + WS_OT, 4 * MB, (((rg * 16 + wave * 4 + i) * 128 + h * 4) * 16 + lane) * 16, o);
    }
  } else {
    int cb = b - 256, l56 = lane < 56 ? lane : 55;
    const char* wp = (const char*)(cb >> 7 ? wu : wg) + (size_t)(((cb >> 3) & 15) * 64 + wave * 16) * FF * 2 + (cb & 7) * 896 + l56 * 16;
    u32x4 v[16];
#pragma unroll
    for (int i = 0; i < 16; ++i) v[i] = *(const u32x4*)(wp + i * FF * 2);
    f32x4 a[2];
#pragma unroll
    for (int c = 0; c < 4; ++c) {
      float lo = 0, hi = 0;
#pragma unroll
      for (int i = 0; i < 16; ++i) { lo += bf_lo(v[i][c]); hi += bf_hi(v[i][c]); }
      a[c >> 1][(c & 1) * 2] = lo; a[c >> 1][(c & 1) * 2 + 1] = hi;
    }
    *LDS3(f32x4, lds0 + wave * 2048 + lane * 32) = a[0];
    *LDS3(f32x4, lds0 + wave * 2048 + lane * 32 + 16) = a[1];
    __syncthreads();
    if (tid < 112) {
      f32x4 s = (*LDS3(f32x4, lds0 + tid * 16) + *LDS3(f32x4, lds0 + 2048 + tid * 16)) + (*LDS3(f32x4, lds0 + 4096 + tid * 16) + *LDS3(f32x4, lds0 + 6144 + tid * 16));
      st16<(WTM & 2) != 0>(ws + WS_CS, 16 * 2 * FF * 4, (((cb >> 3) & 15) * 2 * FF + (cb >> 7) * FF + (cb & 7) * 448 + tid * 4) * 4, __builtin_bit_cast(u32x4, s));
    }
  }
  STAMPW(1, 2);
}

template <int NK> using CR = Cfg<128, 64, 64, 6, 4, 1, 2, NK, 0, 0>;
template <int MODE>
__global__ __launch_bounds__(512, 2) void kR(const void* __restrict__ w, const void* __restrict__ xres, void* __restrict__ out, char* __restrict__ ws) {
  constexpr int K = MODE ? FF : DM;
  using C = CR<K / 64>;
  STAMP(2 + 2 * MODE, 0);
  __shared__ __attribute__((aligned(256))) char sm[C::LDS];
  unsigned lds0 = (unsigned)(size_t)sm;
  int tid = threadIdx.x, lane = tid & 63, wave = __builtin_amdgcn_readfirstlane(tid >> 6);
  int wk = wave & 1, wm = wave >> 1;
  int b = blockIdx.x, xcd = b & 7, idx = b >> 3;
  int tm = (xcd >> 1) * 4 + (idx & 3), tn = (xcd & 1) * 8 + (idx >> 2);
  int g = lane >> 4, r15 = lane & 15;
  int mf = tm * 8 + wm * 2 + wk, row = mf * 16 + r15;
  u32x4 res[2];
#pragma unroll
  for (int jp = 0; jp < 2; ++jp) {
    int c8 = tn * 8 + 2 * (jp * 2 + (g & 1)) + (g >> 1);
    if (MODE == 0) res[jp] = *(const u32x4*)((const char*)xres + ((size_t)row * DM + c8 * 8) * 2);
    else res[jp] = ((const u32x4*)(ws + WS_HT))[(mf * 128 + c8) * 16 + r15];
  }
  Loader<C> L;
  loader_init<C>(L, lds0, wave, lane, ws + (MODE ? WS_ACT : WS_OT), tm * 128, K, w, w, DM, 0, [&](int lc) { return tn * 64 + lc * 8; });
  STAMP3(2, 1);
  prologue<C>(L, wave, lds0);
  STAMP3(2, 2);
  RdAddr<C> R; rd_init<C>(R, lds0, wm, 0, wk, lane, wk, 0, 0);
  f32x4 acc[2][4];
#pragma unroll
  for (int m = 0; m < 2; ++m)
#pragma unroll
    for (int j = 0; j < 4; ++j) acc[m][j] = f32x4{0, 0, 0, 0};
  NoHook hk;
  mainloop<C, 0xfu>(acc, L, R, wave, lds0, hk);
  STAMP(2 + 2 * MODE, 1);
#pragma unroll
  for (int j = 0; j < 4; ++j) *LDS3(f32x4, lds0 + ((wave * 4 + j) * 64 + lane) * 16) = acc[1][j];
  wait_lgkm0(); bar();
#pragma unroll
  for (int j = 0; j < 4; ++j) acc[0][j] += *LDS3(f32x4, lds0 + (((wave ^ 1) * 4 + j) * 64 + lane) * 16);
  float ss = 0, sq = 0;
#pragma unroll
  for (int jp = 0; jp < 2; ++jp) {
    f32x4 a = acc[0][2 * jp], c = acc[0][2 * jp + 1];
#pragma unroll
    for (int r = 0; r < 4; ++r) { float u = a[r], v = c[r]; swap16(u, v); a[r] = u; c[r] = v; }
    u32x4 o;
    o[0] = pack_bf16(a[0] + bf_lo(res[jp][0]), a[1] + bf_hi(res[jp][0]));
    o[1] = pack_bf16(a[2] + bf_lo(res[jp][1]), a[3] + bf_hi(res[jp][1]));
    o[2] = pack_bf16(c[0] + bf_lo(res[jp][2]), c[1] + bf_hi(res[jp][2]));
    o[3] = pack_bf16(c[2] + bf_lo(res[jp][3]), c[3] + bf_hi(res[jp][3]));
    int c8 = tn * 8 + 2 * (jp * 2 + (g & 1)) + (g >> 1);
    if (MODE == 0) {
      st16<(WTM & 4) != 0>(ws + WS_HT, 4 * MB, ((mf * 128 + c8) * 16 + r15) * 16, o);
#pragma unroll
      for (int i = 0; i < 4; ++i) { ss = dot2(o[i], ONES2, ss); sq = dot2(o[i], o[i], sq); }
    } else {
      *(u32x4*)((char*)out + ((size_t)row * DM + c8 * 8) * 2) = o;
    }
  }
  if (MODE == 0) {
    ss = sum_g(ss); sq = sum_g(sq);
    if (g == 0) ((f32x2*)(ws + WS_ST))[row * 16 + tn] = f32x2{ss, sq};
  }
  STAMPW(2 + 2 * MODE, 2);
}

using CD = Cfg<256, 224, 32, 4, 4, 2, 1, 32, 0, 1>;
DEVI float geglu(float x, float u) {
  constexpr float K1 = -2.f * 0.7978845608028654f * 1.4426950408889634f, K2 = K1 * 0.044715f;
  float e = __builtin_amdgcn_exp2f(x * (K1 + K2 * x * x));
  return x * __builtin_amdgcn_rcpf(1.f + e) * u;
}
struct HookD : PfHook<1> {
  char* xch; unsigned xo; int k2;
  DEVI void tail() const {
    glds4(v, p, l);
  }
  DEVI void mid(int kt) const {
  }
};
__global__ __launch_bounds__(512, 2) void kD(const void* __restrict__ wg, const void* __restrict__ wu, const void* __restrict__ wd, char* __restrict__ ws) {
  using C = CD;
  STAMP(3, 0);
  __shared__ __attribute__((aligned(256))) char sm[C::LDS + 4096];
  unsigned lds0 = (unsigned)(size_t)sm;
  constexpr unsigned O_ROW = C::LDS, O_CS = C::LDS + 2048;
  int tid = threadIdx.x, lane = tid & 63, wave = __builtin_amdgcn_readfirstlane(tid >> 6);
  int wn = wave & 1, wm = wave >> 1;
  int b = blockIdx.x, xcd = b & 7, idx = b >> 3;
  int tm = (xcd >> 2) * 4 + (idx & 3), tn = (xcd & 3) * 8 + (idx >> 2);
  {
    Loader<C> L;
    loader_init<C>(L, lds0, wave, lane, ws + WS_HT, tm * 256, DM, wg, wu, FF, 0, [&](int lc) { return tn * 112 + lc * 8; });
    const char* sp = ws + WS_ST + (size_t)(tm * 256 + wave * 32) * 128 + lane * 16;
    f32x4 sv[4];
#pragma unroll
    for (int i = 0; i < 4; ++i) sv[i] = gload4(sp + i * 1024);
    int ct = tid < 448 ? tid >> 1 : 223;
    const float* cp = (const float*)(ws + WS_CS) + (tid & 1) * 16 * FF + (ct < 112 ? tn * 112 + ct : FF + tn * 112 + ct - 112);
    float cv[8];
#pragma unroll
    for (int i = 0; i < 8; ++i) cv[i] = gload1(cp + i * 2 * FF);
    STAMP3(3, 1);
    prologue<C>(L, wave, lds0);
    STAMP3(3, 2);
    wait_stages<C, C::S - 1>(wave);
    asm volatile("" : "+v"(sv[0]), "+v"(sv[1]), "+v"(sv[2]), "+v"(sv[3]), "+v"(cv[0]), "+v"(cv[1]), "+v"(cv[2]), "+v"(cv[3]), "+v"(cv[4]), "+v"(cv[5]), "+v"(cv[6]), "+v"(cv[7]));
    {
      float s[4], q[4];
#pragma unroll
      for (int i = 0; i < 4; ++i) { s[i] = sv[i][0] + sv[i][2]; q[i] = sv[i][1] + sv[i][3]; }
      bool o1 = lane & 1, o2 = lane & 2;
      float sa = (o1 ? s[1] : s[0]) + __shfl_xor(o1 ? s[0] : s[1], 1), sb = (o1 ? s[3] : s[2]) + __shfl_xor(o1 ? s[2] : s[3], 1);
      float qa = (o1 ? q[1] : q[0]) + __shfl_xor(o1 ? q[0] : q[1], 1), qb = (o1 ? q[3] : q[2]) + __shfl_xor(o1 ? q[2] : q[3], 1);
      float ss = (o2 ? sb : sa) + __shfl_xor(o2 ? sa : sb, 2), qq = (o2 ? qb : qa) + __shfl_xor(o2 ? qa : qb, 2);
      ss += __shfl_xor(ss, 4); qq += __shfl_xor(qq, 4);
      float mean = ss * (1.f / DM), var = fmaxf(qq * (1.f / DM) - mean * mean, 0.f);
      float rstd = __builtin_amdgcn_rsqf(var + 1e-5f);
      if (!(lane & 4)) *LDS3(f32x2, lds0 + O_ROW + (wave * 32 + (lane & 3) * 8 + (lane >> 3)) * 8) = f32x2{mean, rstd};
    }
    float cs = ((cv[0] + cv[1]) + (cv[2] + cv[3])) + ((cv[4] + cv[5]) + (cv[6] + cv[7]));
    cs += __shfl_xor(cs, 1);
    if (tid < 448 && !(tid & 1)) *LDS3(float, lds0 + O_CS + ct * 4) = cs;
    RdAddr<C> R; rd_init<C>(R, lds0, wm, wn, 0, lane, 0, 0, 0);
    f32x4 acc[4][7];
#pragma unroll
    for (int m = 0; m < 4; ++m)
#pragma unroll
      for (int j = 0; j < 7; ++j) acc[m][j] = f32x4{0, 0, 0, 0};
    HookD hk; hk.xch = ws + WS_XCH; hk.xo = ((b * 8 + wave) * 256 + lane) * 16; hk.k2 = 4 + (wave & (FST - 1));
    hk.v = lane * 64; hk.l = lds0 + C::LDS + 3072; hk.p = uni_ptr((const char*)wd + (size_t)b * 28672 + (wave < 7 ? wave : 6) * 4096);
    hk.k1 = wave < 7 ? 4 + 3 * wave : -1;
#pragma unroll
    for (int i = 0; i < 4; ++i) st16<FWT>(ws + WS_XCH, 8 * MB, (((b * 8 + wave) * 4 + i) * 64 + lane) * 16, u32x4{~0u, ~0u, ~0u, ~0u});
    mainloop<C, 0x7fu>(acc, L, R, wave, lds0, hk);
    STAMP(3, 1);
    int g = lane >> 4, r15 = lane & 15, hi = g >> 1;
    f32x2 st[2];
#pragma unroll
    for (int p = 0; p < 2; ++p) st[p] = *LDS3(f32x2, lds0 + O_ROW + (wm * 64 + (p * 2 + hi) * 16 + r15) * 8);
    int smf = tm * 16 + wm * 4 + ((g & 1) * 2 + hi);
    u32x4* dst = (u32x4*)(ws + WS_ACT) + ((size_t)smf * (FF / 8) + tn * 14 + wn * 7) * 16 + r15;
    const __amdgpu_buffer_rsrc_t drs = __builtin_amdgcn_make_buffer_rsrc(ws + WS_ACT, 0, SEQ * FF * 2, 0x00020000);
    unsigned doff = ((smf * (FF / 8) + tn * 14 + wn * 7) * 16 + r15) * 16;
#pragma unroll
    for (int j = 0; j < 7; ++j) {
      unsigned ca = lds0 + O_CS + ((wn * 7 + j) * 8 + (g & 1) * 4) * 4;
      f32x4 cg = *LDS3(f32x4, ca), cu = *LDS3(f32x4, ca + 448);
      unsigned pk[2][2];
#pragma unroll
      for (int p = 0; p < 2; ++p) {
        float a[4];
#pragma unroll
        for (int r = 0; r < 4; ++r) {
          float x = acc[2 * p][j][r], y = acc[2 * p + 1][j][r];
          swap32(x, y);
          a[r] = geglu((x - st[p][0] * cg[r]) * st[p][1], (y - st[p][0] * cu[r]) * st[p][1]);
        }
        pk[p][0] = pack_bf16(a[0], a[1]); pk[p][1] = pack_bf16(a[2], a[3]);
      }
      swap16u(pk[0][0], pk[1][0]); swap16u(pk[0][1], pk[1][1]);
      __builtin_amdgcn_raw_buffer_store_b128(u32x4{pk[0][0], pk[0][1], pk[1][0], pk[1][1]}, drs, doff + j * 256, 0, DST);
    }
  }
  STAMPW(3, 2);
}

using CE = Cfg<128, 128, 64, 4, 2, 2, 2, FF / 128, 0, 0>;
typedef __attribute__((address_space(1))) unsigned gu32;
__global__ __launch_bounds__(512, 2) void kE(const void* __restrict__ w, void* __restrict__ out, char* __restrict__ ws) {
  using C = CE;
  STAMP(4, 0);
  __shared__ __attribute__((aligned(256))) char sm[C::LDS];
  unsigned lds0 = (unsigned)(size_t)sm;
  int tid = threadIdx.x, lane = tid & 63, wave = __builtin_amdgcn_readfirstlane(tid >> 6);
  int wk = wave & 1, wn = (wave >> 1) & 1, wm = wave >> 2;
  int b = blockIdx.x, xcd = b & 7, idx = b >> 3;
  int kh = xcd & 1, tm = (xcd >> 1) * 4 + (idx & 3), tn = idx >> 2;
  int g = lane >> 4, r15 = lane & 15;
  int c8 = tn * 16 + wn * 8 + 2 * (2 * wk + (g & 1)) + (g >> 1);
  int mf0 = tm * 8 + wm * 4 + 2 * kh;
  u32x4 res[2];
#pragma unroll
  for (int m = 0; m < 2; ++m) res[m] = ((const u32x4*)(ws + WS_HT))[((mf0 + m) * 128 + c8) * 16 + r15];
  Loader<C> L;
  loader_init<C>(L, lds0, wave, lane, ws + WS_ACT, tm * 128, FF, w, w, DM, kh * (FF / 2), [&](int lc) { return tn * 128 + lc * 8; }, 2 * kh);
  STAMP3(4, 1);
  prologue<C>(L, wave, lds0);
  STAMP3(4, 2);
  RdAddr<C> R; rd_init<C>(R, lds0, wm, wn, wk, lane, 0, 0, 2 * wk);
  f32x4 acc[4][4];
#pragma unroll
  for (int m = 0; m < 4; ++m)
#pragma unroll
    for (int j = 0; j < 4; ++j) acc[m][j] = f32x4{0, 0, 0, 0};
  NoHook hk;
  mainloop<C, 0xfu>(acc, L, R, wave, lds0, hk);
  STAMP(4, 1);
#pragma unroll
  for (int m = 3; m >= 0; --m)
#pragma unroll
    for (int j = 0; j < 2; ++j) *LDS3(f32x4, lds0 + ((wave * 8 + m * 2 + j) * 64 + lane) * 16) = acc[m][2 + j];
  wait_lgkm0(); bar();
  const __amdgpu_buffer_rsrc_t rs = __builtin_amdgcn_make_buffer_rsrc(ws + WS_XCH, 0, 256 * 8 * 4096, 0x00020000);
#pragma unroll
  for (int m = 3; m >= 0; --m)
#pragma unroll
    for (int j = 0; j < 2; ++j) {
      acc[m][j] += *LDS3(f32x4, lds0 + (((wave ^ 1) * 8 + m * 2 + j) * 64 + lane) * 16);
      if (m >= 2) {
        u32x4 u = __builtin_bit_cast(u32x4, acc[m][j]);
#pragma unroll
        for (int r = 0; r < 4; ++r) u[r] = min(u[r], 0xfffffffeu);
        __builtin_amdgcn_raw_buffer_store_b128(u, rs, (((b * 8 + wave) * 4 + (m - 2) * 2 + j) * 64 + lane) * 16, 0, 16);
      }
    }
  u32x4 x[4];
  if (ESL0) __builtin_amdgcn_s_sleep(ESL0);
  for (int spin = 0; spin < (1 << 20); ++spin) {
    asm volatile("" ::: "memory");
#pragma unroll
    for (int i = 0; i < 4; ++i) x[i] = __builtin_amdgcn_raw_buffer_load_b128(rs, ((((b ^ 1) * 8 + wave) * 4 + i) * 64 + lane) * 16, 0, 16);
    unsigned mx = 0;
#pragma unroll
    for (int i = 0; i < 4; ++i) mx = max(max(mx, max(x[i][0], x[i][1])), max(x[i][2], x[i][3]));
    if (__builtin_amdgcn_ballot_w64(mx == 0xffffffffu) == 0) break;
    if (ESL) __builtin_amdgcn_s_sleep(ESL);
  }
#pragma unroll
  for (int m = 0; m < 2; ++m) {
    f32x4 a = acc[m][0] + __builtin_bit_cast(f32x4, x[2 * m]), c = acc[m][1] + __builtin_bit_cast(f32x4, x[2 * m + 1]);
#pragma unroll
    for (int r = 0; r < 4; ++r) { float u = a[r], v = c[r]; swap16(u, v); a[r] = u; c[r] = v; }
    u32x4 o;
    o[0] = pack_bf16(a[0] + bf_lo(res[m][0]), a[1] + bf_hi(res[m][0]));
    o[1] = pack_bf16(a[2] + bf_lo(res[m][1]), a[3] + bf_hi(res[m][1]));
    o[2] = pack_bf16(c[0] + bf_lo(res[m][2]), c[1] + bf_hi(res[m][2]));
    o[3] = pack_bf16(c[2] + bf_lo(res[m][3]), c[3] + bf_hi(res[m][3]));
    st16<(WTM & 8) != 0>(out, 4 * MB, (((mf0 + m) * 16 + r15) * DM + c8 * 8) * 2, o);
  }
  STAMPW(4, 2);
}

extern "C" void kernel_launch(void* const* d_in, const int* in_sizes, int n_in, void* d_out, int out_size, void* d_ws, size_t ws_size,
                              hipStream_t stream) {
  char* ws = (char*)d_ws;
  kA<<<256, 512, 0, stream>>>(d_in[0], d_in[1], d_in[2], d_in[4], d_in[5], d_in[6], ws);
  kB<<<CSA ? 256 : 512, 256, 0, stream>>>(d_in[4], d_in[5], ws);
  kR<0><<<256, 512, 0, stream>>>(d_in[2], d_in[0], nullptr, ws);
  kD<<<256, 512, 0, stream>>>(d_in[4], d_in[5], d_in[6], ws);
  kE<<<256, 512, 0, stream>>>(d_in[6], d_out, ws);
}
```

```cpp
#include <hip/hip_runtime.h>
#include <stdint.h>
#define STAGE_D 0
#define ASC 0
#define UMAP 1
#define STAG 2

typedef __attribute__((ext_vector_type(4))) short s16x4;
typedef __attribute__((ext_vector_type(8))) short s16x8;
typedef __attribute__((ext_vector_type(8))) __bf16 bf16x8;
typedef __attribute__((ext_vector_type(2))) __bf16 bf16x2;
typedef __attribute__((ext_vector_type(4))) float f32x4;
typedef __attribute__((ext_vector_type(2))) float f32x2;
typedef __attribute__((ext_vector_type(4))) unsigned u32x4;
typedef __attribute__((ext_vector_type(2))) unsigned u32x2;
#define LDS3(T, a) ((__attribute__((address_space(3))) T*)(size_t)(a))
#define DEVI __device__ __forceinline__

template <int N> DEVI void wait_vm() { asm volatile("s_waitcnt vmcnt(%0)" ::"n"(N) : "memory"); }
DEVI void wait_lgkm0() { asm volatile("s_waitcnt lgkmcnt(0)" ::: "memory"); }
DEVI void bar() { __builtin_amdgcn_s_barrier(); }
DEVI void glds16(unsigned voff, const char* sbase, unsigned lds) {
  asm volatile("s_mov_b32 m0, %2\n\ts_nop 0\n\tglobal_load_lds_dwordx4 %0, %1" ::"v"(voff), "s"(sbase), "s"(lds) : "memory");
}
DEVI void glds16c(unsigned voff, const char* sbase, unsigned lds) {
  asm volatile("s_mov_b32 m0, %2\n\ts_nop 0\n\tglobal_load_lds_dwordx4 %0, %1 sc1" ::"v"(voff), "s"(sbase), "s"(lds) : "memory");
}
DEVI bf16x8 lds_b128(unsigned a) { return *LDS3(bf16x8, a); }
DEVI s16x4 lds_tr(unsigned a) { return __builtin_amdgcn_ds_read_tr16_b64_v4i16(LDS3(s16x4, a)); }
DEVI unsigned pack_bf16(float lo, float hi) {
  bf16x2 v; v[0] = (__bf16)lo; v[1] = (__bf16)hi; return __builtin_bit_cast(unsigned, v);
}
DEVI float bf_lo(unsigned u) { return __uint_as_float(u << 16); }
DEVI float bf_hi(unsigned u) { return __uint_as_float(u & 0xffff0000u); }
DEVI float dot2(unsigned a, unsigned b, float c) {
  return __builtin_amdgcn_fdot2_f32_bf16(__builtin_bit_cast(bf16x2, a), __builtin_bit_cast(bf16x2, b), c, false);
}
DEVI void swap16(float& a, float& b) {
  auto r = __builtin_amdgcn_permlane16_swap(__float_as_uint(a), __float_as_uint(b), false, false);
  a = __uint_as_float(r[0]); b = __uint_as_float(r[1]);
}
DEVI void swap16u(unsigned& a, unsigned& b) {
  auto r = __builtin_amdgcn_permlane16_swap(a, b, false, false);
  a = r[0]; b = r[1];
}
DEVI void swap32(float& a, float& b) {
  auto r = __builtin_amdgcn_permlane32_swap(__float_as_uint(a), __float_as_uint(b), false, false);
  a = __uint_as_float(r[0]); b = __uint_as_float(r[1]);
}
DEVI float sum_g(float x) {
  float a = x, b = x; swap16(a, b); x = a + b;
  a = x; b = x; swap32(a, b); return a + b;
}
DEVI f32x4 mfma16(bf16x8 a, bf16x8 b, f32x4 c) { return __builtin_amdgcn_mfma_f32_16x16x32_bf16(a, b, c, 0, 0, 0); }

template <int BM_, int BN_, int BK_, int S_, int WM_, int WN_, int WK_, int NK_, int AMODE_, int BMODE_, int NPF_ = 0>
struct Cfg {
  static constexpr int NPF = NPF_;
  static constexpr int BM = BM_, BN = BN_, BK = BK_, S = S_, WM = WM_, WN = WN_, WK = WK_, NK = NK_, AMODE = AMODE_, BMODE = BMODE_;
  static constexpr int NW = WM * WN * WK;
  static constexpr int MF = BM / WM / 16, NF = BN / WN / 16, KF = BK / 32, KFW = KF / WK;
  static constexpr int A_BYTES = BM * BK * 2;
  static constexpr int B_BYTES = BN * BK * 2 + (BMODE == 1 ? 256 : 0);
  static constexpr int STAGE = A_BYTES + B_BYTES;
  static constexpr int PA = A_BYTES / 1024, PB = BN * BK * 2 / 1024, P = PA + PB;
  static constexpr int NLW = NPF ? NW - 1 : NW;
  static constexpr int NP = (P + NLW - 1) / NLW;
  static constexpr int FULLW = P - (NP - 1) * NLW;
  static constexpr int RS = (BMODE == 1 ? BN : BN * 2);
  static constexpr int LDS = S * STAGE;
  static constexpr bool SLOT = (MF == 2);
};

template <class C> struct Frags { bf16x8 a[C::KFW][C::MF]; bf16x8 b[C::KFW][C::NF]; };
template <class C> struct Loader {
  const char* sbase[C::NP]; unsigned voff[C::NP]; unsigned ldso[C::NP]; unsigned sstr[C::NP];
  const char* pfb[4]; unsigned pfv, pfl, pfs[4];
};
DEVI void glds4(unsigned voff, const char* sbase, unsigned lds) {
  asm volatile("s_mov_b32 m0, %2\n\ts_nop 0\n\tglobal_load_lds_dword %0, %1" ::"v"(voff), "s"(sbase), "s"(lds) : "memory");
}
template <class C> DEVI void issue_pf(Loader<C>& L, int i) { glds4(L.pfv, L.pfb[i], L.pfl); L.pfb[i] += L.pfs[i]; }
DEVI const char* uni_ptr(const char* p) {
  unsigned long long u = (unsigned long long)p;
  unsigned lo = __builtin_amdgcn_readfirstlane((unsigned)u), hi = __builtin_amdgcn_readfirstlane((unsigned)(u >> 32));
  return (const char*)(((unsigned long long)hi << 32) | lo);
}

template <class C> DEVI void issue_piece(Loader<C>& L, int i, int wave, unsigned bufbase) {
  if (C::AMODE == 0 && i < 2) glds16c(L.voff[i], L.sbase[i], bufbase + L.ldso[i]);
  else if (i < C::NP - 1 ? (C::NLW == C::NW || wave < C::NLW) : (C::FULLW == C::NW || wave < C::FULLW)) glds16(L.voff[i], L.sbase[i], bufbase + L.ldso[i]);
  L.sbase[i] += L.sstr[i];
}
template <class C, class ColMap>
DEVI void loader_init(Loader<C>& L, unsigned lds0, int wave, int lane, const void* A, int m0, int lda, const void* B0, const void* B1, int ldb, int k0,
                      ColMap colmap, int mxor = 0, unsigned pmask = ~0u) {
#pragma unroll
  for (int i = 0; i < C::NP; ++i) {
    int p = wave + i * C::NLW;
    if (p >= C::P) p = C::P - 1;
    if (p < C::PA) {
      if (C::AMODE == 0) {
        int mb = p / C::KF, kb = p % C::KF;
        L.sbase[i] = (const char*)A + ((size_t)(m0 / 16 + (mb ^ mxor)) * (lda / 8) + (k0 / 8 + kb * 4)) * 256;
        L.voff[i] = lane * 16;
        L.sstr[i] = C::BK / 8 * 256;
      } else {
        int row = 8 * p + (lane >> 3), pos = lane & 7, lc = pos ^ ((row >> 1) & 7);
        L.sbase[i] = (const char*)A + ((size_t)(m0 + 8 * p) * lda + k0) * 2;
        L.voff[i] = ((lane >> 3) * lda + lc * 8) * 2;
        L.sstr[i] = C::BK * 2;
      }
      L.ldso[i] = p * 1024;
    } else {
      int pb = p - C::PA;
      if (C::BMODE == 0) {
        constexpr int CH = C::BN / 8;
        int ci = pb * 64 + lane, k = ci / CH, c = ci % CH, u = c >> 1;
        int sg = (C::RS % 256 == 0) ? ((k & 3) | (((k >> 3) & 1) << 2)) : (C::RS % 256 == 128) ? (((k >> 1) & 1) | (((k >> 3) & 1) << 1)) : ((k >> 3) & 1);
        int lc = ((u ^ sg) << 1) | (c & 1);
        L.sbase[i] = (const char*)B0 + (size_t)k0 * ldb * 2;
        L.voff[i] = (k * ldb + colmap(lc)) * 2;
        L.ldso[i] = C::A_BYTES + pb * 1024;
      } else {
        constexpr int CH = C::BN / 16, HP = C::PB / 2;
        int half = pb / HP, pp = pb % HP;
        int ci = pp * 64 + lane, k = ci / CH, c = ci % CH, lc = c ^ ((k >> 3) & 1);
        L.sbase[i] = (const char*)(half ? B1 : B0) + (size_t)k0 * ldb * 2;
        L.voff[i] = (k * ldb + colmap(lc)) * 2;
        L.ldso[i] = C::A_BYTES + half * (HP * 1024 + 128) + pp * 1024;
      }
      L.sstr[i] = C::BK * ldb * 2;
    }
    {
      unsigned long long u = (unsigned long long)L.sbase[i];
      unsigned lo = __builtin_amdgcn_readfirstlane((unsigned)u), hi = __builtin_amdgcn_readfirstlane((unsigned)(u >> 32));
      L.sbase[i] = (const char*)(((unsigned long long)hi << 32) | lo);
      L.ldso[i] = __builtin_amdgcn_readfirstlane(L.ldso[i]);
      L.sstr[i] = __builtin_amdgcn_readfirstlane(L.sstr[i]);
    }
    if ((pmask >> i) & 1) issue_piece<C>(L, i, wave, lds0);
  }
}

template <class C, int NST, int X = 0> DEVI void wait_stages(int wave) {
  if (C::FULLW == C::NW || wave < C::FULLW) wait_vm<NST * C::NP + X>();
  else if (C::NLW == C::NW || wave < C::NLW) wait_vm<NST*(C::NP - 1) + X>();
}

template <class C> struct RdAddr { unsigned a[C::KFW][2]; unsigned b[C::NF]; unsigned kbo[2]; };
template <class C> DEVI void rd_init(RdAddr<C>& R, unsigned lds0, int wm, int wn, int wk, int lane, int mx, int kx, int jx) {
  int g = lane >> 4, r15 = lane & 15, q = (lane & 15) >> 2, p = lane & 3;
#pragma unroll
  for (int kb = 0; kb < C::KFW; ++kb)
#pragma unroll
    for (int m = 0; m < 2; ++m) {
      int ka = wk * C::KFW + (kb ^ kx), ma = wm * C::MF + (m ^ mx);
      if (C::AMODE == 0) R.a[kb][m] = lds0 + (ma * C::KF + ka) * 1024 + lane * 16;
      else R.a[kb][m] = lds0 + (ma * 16 + r15) * 128 + (((ka * 4 + g) ^ ((r15 >> 1) & 7)) * 16);
    }
  R.kbo[0] = kx * 32 * C::RS; R.kbo[1] = (1 ^ kx) * 32 * C::RS;
  int krow = wk * C::KFW * 32 + 8 * g + q;
#pragma unroll
  for (int j = 0; j < C::NF; ++j) {
    int U = wn * C::NF + (j ^ jx);
    if (UMAP && C::AMODE == 1) U = (j >> 1) * 4 + wn * 2 + ((j ^ jx) & 1);
    if (C::BMODE == 0) {
      int sg = (C::RS % 256 == 0) ? (q | ((g & 1) << 2)) : (C::RS % 256 == 128) ? ((q >> 1) | ((g & 1) << 1)) : (g & 1);
      R.b[j] = lds0 + C::A_BYTES + krow * C::RS + ((U ^ sg) * 32) + p * 8;
    } else {
      R.b[j] = lds0 + C::A_BYTES + (p >> 1) * (C::PB / 2 * 1024 + 128) + krow * C::RS + ((U ^ (g & 1)) * 16) + (p & 1) * 8;
    }
  }
}
template <class C> DEVI void read_a(Frags<C>& f, const RdAddr<C>& R, unsigned so, int kb, int mf) {
  if (C::SLOT) f.a[kb][mf] = lds_b128(R.a[kb][mf & 1] + so);
  else f.a[kb][mf] = lds_b128(R.a[0][0] + so + (mf * C::KF + kb) * 1024);
}
template <class C> DEVI void read_b(Frags<C>& f, const RdAddr<C>& R, unsigned so, int kb, int j) {
  unsigned a = (C::AMODE == 1) ? R.b[j] + (so + R.kbo[kb & 1]) : R.b[j] + so + (kb * 32) * C::RS;
  s16x4 lo = lds_tr(a);
  s16x4 hi = lds_tr(a + 4 * C::RS);
  s16x8 v = __builtin_shufflevector(lo, hi, 0, 1, 2, 3, 4, 5, 6, 7);
  f.b[kb][j] = __builtin_bit_cast(bf16x8, v);
}
template <class C> DEVI void read_all(Frags<C>& f, const RdAddr<C>& R, unsigned so) {
#pragma unroll
  for (int kb = 0; kb < C::KFW; ++kb) {
#pragma unroll
    for (int m = 0; m < C::MF; ++m) read_a<C>(f, R, so, kb, m);
#pragma unroll
    for (int j = 0; j < C::NF; ++j) read_b<C>(f, R, so, kb, j);
  }
}

template <class C, unsigned SWAPMASK, bool ISSUE, bool READ, int NOUT, int PF, class Hook>
DEVI void iter(f32x4 (&acc)[C::MF][C::NF], const Frags<C>& cur, Frags<C>& nxt, Loader<C>& L, const RdAddr<C>& R, int wave, unsigned lds0,
               unsigned& bcur, Hook& hook, int kt) {
  wait_lgkm0();
  if (READ) { wait_stages<C, NOUT, (PF == 10 ? Hook::NT : 0)>(wave); }
  bar();
  if (PF == 9) hook.tail();
  unsigned bn = bcur + C::STAGE; if (bn == C::LDS) bn = 0;
  constexpr int NM = C::KFW * C::MF * C::NF;
  constexpr int NR = C::KFW * (C::MF + C::NF);
  constexpr int SEG = C::NP + 1;
#pragma unroll
  for (int s = 0; s < SEG; ++s) {
    if (READ) {
#pragma unroll
      for (int r = s * NR / SEG; r < (s + 1) * NR / SEG; ++r) {
        int kb = r / (C::MF + C::NF), x = r % (C::MF + C::NF);
        if (x < C::MF) read_a<C>(nxt, R, bn, kb, x); else read_b<C>(nxt, R, bn, kb, x - C::MF);
      }
    }
#pragma unroll
    for (int i = s * NM / SEG; i < (s + 1) * NM / SEG; ++i) {
      int kb = i / (C::MF * C::NF), x = i % (C::MF * C::NF), j = x / C::MF, m = x % C::MF;
      if ((SWAPMASK >> j) & 1) acc[m][j] = mfma16(cur.b[kb][j], cur.a[kb][m], acc[m][j]);
      else acc[m][j] = mfma16(cur.a[kb][m], cur.b[kb][j], acc[m][j]);
    }
    hook.seg(cur, s, SEG);
#pragma unroll
    for (int x = 0; x < 8; ++x) {
      __builtin_amdgcn_sched_group_barrier(0x8, 1, 0);
      __builtin_amdgcn_sched_group_barrier(0x100, 1, 0);
      if (Hook::VALU) __builtin_amdgcn_sched_group_barrier(0x2, Hook::VALU, 0);
    }
    __builtin_amdgcn_sched_barrier(0);
    if (PF && PF < 9 && (s == 1 || s == 3) && (PF - 1) * 2 + (s >> 1) < C::NPF) { if (wave == C::NW - 1) issue_pf<C>(L, (PF - 1) * 2 + (s >> 1)); }
    if (ISSUE && s < C::NP) { issue_piece<C>(L, s, wave, lds0 + bcur); __builtin_amdgcn_sched_barrier(0); }
    if (s == SEG - 1) hook.mid(kt);
  }
  bcur = bn;
}

template <class C> DEVI void prologue(Loader<C>& L, int wave, unsigned lds0, int s0 = 1, unsigned pmask = ~0u) {
#pragma unroll
  for (int s = s0; s < C::S; ++s) {
#pragma unroll
    for (int i = 0; i < C::NP; ++i) if ((pmask >> i) & 1) issue_piece<C>(L, i, wave, lds0 + s * C::STAGE);
  }
}

struct NoHook { static constexpr int VALU = 0, NT = 0; template <class F> DEVI void seg(const F&, int, int) const {} DEVI void tail() const {} DEVI void mid(int) const {} };
template <int N> struct XHook : NoHook { static constexpr int NT = N; };
template <class C, unsigned SWAPMASK, class Hook, bool PEEL = false>
DEVI void mainloop(f32x4 (&acc)[C::MF][C::NF], Loader<C>& L, const RdAddr<C>& R, int wave, unsigned lds0, Hook& hook) {
  constexpr int S = C::S, NK = C::NK;
  Frags<C> f0, f1;
  if (PEEL) wait_vm<2 * (S - 1)>(); else wait_stages<C, S - 1>(wave);
  bar();
  read_all<C>(f0, R, 0);
  unsigned bcur = 0;
  constexpr int NMAIN = NK - S;
  constexpr int NPAIR = NMAIN / 2;
  if constexpr (PEEL) {
    static_assert(S == 4 && C::PA == 16 && C::NW == 8, "piece layout");
    XHook<4> h0; XHook<2> h1;
    iter<C, SWAPMASK, true, true, 0, 10>(acc, f0, f1, L, R, wave, lds0, bcur, h0, 0);
    iter<C, SWAPMASK, true, true, 1, 10>(acc, f1, f0, L, R, wave, lds0, bcur, h1, 1);
  }
  for (int it = PEEL; it < NPAIR; ++it) {
    iter<C, SWAPMASK, true, true, S - 2, (C::NPF > 0 ? 1 : 0)>(acc, f0, f1, L, R, wave, lds0, bcur, hook, 2 * it);
    iter<C, SWAPMASK, true, true, S - 2, (C::NPF > 2 ? 2 : 0)>(acc, f1, f0, L, R, wave, lds0, bcur, hook, 2 * it + 1);
  }
  constexpr int T0 = 2 * NPAIR;
#define TAILIT(T, CUR, NXT)                                                        \
  if constexpr (T0 + (T) < NK) {                                                   \
    constexpr int kt = T0 + (T);                                                   \
    constexpr bool iss = kt + S < NK, rd = kt + 1 < NK;                            \
    constexpr int last_issued = (kt + S - 1 < NK - 1) ? kt + S - 1 : NK - 1;       \
    constexpr int nout = rd ? last_issued - (kt + 1) : 0;                          \
    iter<C, SWAPMASK, iss, rd, nout, (kt == NK - S ? 9 : kt > NK - S ? 10 : 0)>(acc, CUR, NXT, L, R, wave, lds0, bcur, hook, kt); \
  }
  TAILIT(0, f0, f1) TAILIT(1, f1, f0) TAILIT(2, f0, f1) TAILIT(3, f1, f0) TAILIT(4, f0, f1) TAILIT(5, f1, f0) TAILIT(6, f0, f1) TAILIT(7, f1, f0)
#undef TAILIT
}
template <int N> struct PfHook {
  static constexpr int VALU = 0, NT = (STAGE_D ? 0 : N);
  const char* p; unsigned v, l; int k1;
  template <class F> DEVI void seg(const F&, int, int) const {}
  DEVI void tail() const {
#pragma unroll
    for (int i = 0; i < N; ++i) glds4(v, p + i * 4096, l);
  }
  DEVI void mid(int kt) const {
  }
};

#define FILL 1
#define ESL 1
#define ESL0 40
#define FST 4
#define FWT 1
#define FUSE 1
#define CFENCE 1
#define CSL0 8
#define TNX 0
#define LINPF 2
#define DST 16
#define CSA 1
#define STAGD 4
#define STAG0 3
constexpr int SEQ = 2048, DM = 1024, FF = 3584;
constexpr size_t MB = 1u << 20;
constexpr size_t WS_QF = 0;
constexpr size_t WS_OT = 4 * MB;
constexpr size_t WS_HT = 8 * MB;
constexpr size_t WS_KVP = 12 * MB;
constexpr size_t WS_ZP = 14 * MB;
constexpr size_t WS_ST = 14 * MB + 256 * 1024;
constexpr size_t WS_CS = 14 * MB + 768 * 1024;
constexpr size_t WS_ACT = 16 * MB;
constexpr size_t WS_FLG = 15 * MB + 512 * 1024;
constexpr size_t WS_XCH = 32 * MB;
#define DIAG3 0
#define STAMP3(kid, pos)
#define STAMP(kid, i)
#define STAMPW(kid, i)
typedef __attribute__((address_space(1))) unsigned gu32;
constexpr unsigned ONES2 = 0x3f803f80u;
#define WTM 15
template <bool WT> DEVI void st16(void* base, unsigned bytes, unsigned off, u32x4 v) {
  if (WT) __builtin_amdgcn_raw_buffer_store_b128(v, __builtin_amdgcn_make_buffer_rsrc(base, 0, bytes, 0x00020000), off, 0, 16);
  else *(u32x4*)((char*)base + off) = v;
}

DEVI f32x4 gload4(const void* p) { f32x4 r; asm volatile("global_load_dwordx4 %0, %1, off" : "=v"(r) : "v"(p) : "memory"); return r; }
DEVI f32x4 gload4c(const void* p) { f32x4 r; asm volatile("global_load_dwordx4 %0, %1, off sc1" : "=v"(r) : "v"(p) : "memory"); return r; }
DEVI float gload1(const void* p) { float r; asm volatile("global_load_dword %0, %1, off" : "=v"(r) : "v"(p) : "memory"); return r; }
DEVI float phi(float t) { return (t > 0.f ? t + 1.f : __expf(t)) + 1e-6f; }

#define SV 0
#define SVV 1
#define SA 3
using CA = Cfg<128, 192, 64, SA, 4, 2, 1, 16, 1, 0, 0>;
DEVI void touch(unsigned& d, unsigned voff, const char* sbase, int a, int b) {
  asm volatile("s_cmp_lg_u32 %3, %4\n\ts_cbranch_scc1 .Ltch%=\n\tglobal_load_dword %0, %1, %2\n.Ltch%=:" : "+v"(d) : "v"(voff), "s"(sbase), "s"(a), "s"(b) : "memory", "scc");
}
struct HookA {
  static constexpr int VALU = SV ? SVV : 0, NT = 1;
  f32x4 rs, rq, c[3];
  const char* p; unsigned v, v2, d; int k1;
  char* xch; unsigned xo;
  DEVI void fill() const {
#pragma unroll
    for (int i = 0; i < 4; ++i) st16<FWT>(xch, 8 * MB, xo + i * 1024, u32x4{~0u, ~0u, ~0u, ~0u});
  }
  DEVI void tail() { touch(d, v2, p, k1, k1); if (FILL == 5) fill(); }
  DEVI void mid(int kt) {
    if (kt < CA::NK - CA::S) touch(d, v, p, kt, k1);
  }
  template <bool V> DEVI void acc1(f32x4& r, bf16x8 x, bool sqr) {
    bf16x8 ones = __builtin_bit_cast(bf16x8, u32x4{ONES2, ONES2, ONES2, ONES2});
    if (V) {
      u32x4 u = __builtin_bit_cast(u32x4, x);
#pragma unroll
      for (int i = 0; i < 4; ++i) r[i & 1] = dot2(u[i], sqr ? u[i] : ONES2, r[i & 1]);
    } else r = mfma16(sqr ? x : ones, x, r);
  }
  template <class F> DEVI void seg(const F& f, int s, int SEG) {
#pragma unroll
    for (int u = s * 7 / SEG; u < (s + 1) * 7 / SEG; ++u) {
      if (u == 0) acc1<(SV & 1) != 0>(rs, f.a[0][0], false);
      if (u == 1) acc1<(SV & 2) != 0>(rq, f.a[0][0], true);
      if (u == 2) acc1<(SV & 4) != 0>(c[0], f.b[0][0], false);
      if (u == 3) acc1<(SV & 4) != 0>(c[1], f.b[0][2], false);
      if (u == 4) acc1<(SV & 4) != 0>(c[2], f.b[0][4], false);
      if (u == 5) acc1<(SV & 1) != 0>(rs, f.a[1][0], false);
      if (u == 6) acc1<(SV & 2) != 0>(rq, f.a[1][0], true);
    }
  }
};

__global__ __launch_bounds__(512, 2) void kA(const void* __restrict__ x, const void* __restrict__ wqkv, const void* __restrict__ wo, const void* __restrict__ wg,
                                             const void* __restrict__ wu, const void* __restrict__ wd, char* __restrict__ ws) {
  using C = CA;
  STAMP(0, 0);
  __shared__ __attribute__((aligned(256))) char sm[C::LDS];
  unsigned lds0 = (unsigned)(size_t)sm;
  int tid = threadIdx.x, lane = tid & 63, wave = __builtin_amdgcn_readfirstlane(tid >> 6);
  int wn = wave & 1, wm = wave >> 1;
  int b = blockIdx.x, xcd = b & 7, idx = b >> 3;
  int tm = (xcd >> 1) * 4 + (idx & 3), tn = (xcd & 1) * 8 + ((idx >> 2) ^ TNX);
  Loader<C> L;
  loader_init<C>(L, lds0, wave, lane, x, tm * 128, DM, wqkv, wqkv, 3 * DM, 0, [&](int lc) {
    return (lc >> 3) * DM + tn * 64 + (lc & 7) * 8;
  });
  STAMP3(0, 1);
  prologue<C>(L, wave, lds0);
  STAMP3(0, 2);
  HookA hk; hk.d = 0; hk.xch = ws + WS_XCH; hk.xo = ((b * 8 + wave) * 256 + lane) * 16;
  touch(hk.d, lane * 64, uni_ptr((const char*)wqkv + (size_t)b * 24576 + (wave < 6 ? wave : 0) * 4096), (wave + 2) >> 3, 0);
  int mx = wn, kx = wm & 1, jx = wm >> 1;
  RdAddr<C> R; rd_init<C>(R, lds0, wm, wn, 0, lane, mx, kx, jx);
  f32x4 acc[2][6];
#pragma unroll
  for (int m = 0; m < 2; ++m)
#pragma unroll
    for (int j = 0; j < 6; ++j) acc[m][j] = f32x4{0, 0, 0, 0};
  const char* wp = uni_ptr((const char*)(b >> 7 ? wu : wg) + (size_t)((b >> 3) & 15) * 64 * FF * 2 + (b & 7) * 896);
  {
    int i1 = wave * 128 + lane, i2 = i1 + 64;
    hk.p = wave < 7 ? wp : uni_ptr((const char*)wo + (size_t)b * 8192);
    hk.v = wave < 7 ? (i1 / 14) * (FF * 2) + (i1 % 14) * 64 : lane * 64;
    hk.v2 = wave < 7 ? (i2 / 14) * (FF * 2) + (i2 % 14) * 64 : lane * 64 + 4096;
    hk.k1 = STAG0 + wave;
  }
  hk.rs = hk.rq = hk.c[0] = hk.c[1] = hk.c[2] = f32x4{0, 0, 0, 0};
  if (FILL == 7) hk.fill();
  mainloop<C, 0x3u>(acc, L, R, wave, lds0, hk);
  STAMP(0, 1);
  if (tid < 3) __hip_atomic_store((gu32*)(ws + WS_FLG + tid * 2048 + (b >> 5) * 256 + (b & 31) * 4), 0u, __ATOMIC_RELAXED, __HIP_MEMORY_SCOPE_AGENT);
  u32x4 wv[8];
  if (CSA) {
    int l56 = lane < 56 ? lane : 55;
#pragma unroll
    for (int i = 0; i < 8; ++i) wv[i] = *(const __attribute__((address_space(1))) u32x4*)(size_t)(wp + (wave * 8 + i) * (FF * 2) + l56 * 16);
  }
  int g = lane >> 4, r15 = lane & 15;
  constexpr unsigned O_ROW = 0, O_CS = 1024, O_Z = 4096, O_RED = 8192, O_WP = 40960;
  {
    int rr = r15 & 3;
    float s = hk.rs[0], q = rr == 0 ? hk.rq[0] : rr == 1 ? hk.rq[1] : rr == 2 ? hk.rq[2] : hk.rq[3];
    if (SV & 1) s = sum_g(hk.rs[0] + hk.rs[1]);
    if (SV & 2) q = sum_g(hk.rq[0] + hk.rq[1]);
    if (SV & 4) {
#pragma unroll
      for (int i = 0; i < 3; ++i) hk.c[i][0] = sum_g(hk.c[i][0] + hk.c[i][1]);
    }
    float mean = s * (1.f / DM), var = fmaxf(q * (1.f / DM) - mean * mean, 0.f);
    float rstd = __builtin_amdgcn_rsqf(var + 1e-5f);
    if (g == (r15 >> 2)) *LDS3(f32x2, lds0 + O_ROW + (wm * 32 + wn * 16 + r15) * 8) = f32x2{mean, rstd};
    float cc = g == 1 ? hk.c[0][0] : g == 2 ? hk.c[1][0] : hk.c[2][0];
    if (g) *LDS3(float, lds0 + O_CS + (kx * 192 + wn * 96 + (((g - 1) * 2) ^ jx) * 16 + r15) * 4) = cc;
  }
  wait_lgkm0(); bar();
  int head = 2 * tn + wn;
#pragma unroll
  for (int m = 0; m < 2; ++m) {
    int ma = m ^ mx;
    f32x2 st = *LDS3(f32x2, lds0 + O_ROW + (wm * 32 + ma * 16 + r15) * 8);
    unsigned pk[2][2];
#pragma unroll
    for (int j = 0; j < 2; ++j) {
      unsigned ca = lds0 + O_CS + (wn * 96 + (j ^ jx) * 16 + 4 * g) * 4;
      f32x4 cs = *LDS3(f32x4, ca) + *LDS3(f32x4, ca + 768);
      float v[4];
#pragma unroll
      for (int r = 0; r < 4; ++r) v[r] = phi((acc[m][j][r] - st[0] * cs[r]) * st[1]);
      pk[j][0] = pack_bf16(v[0], v[1]); pk[j][1] = pack_bf16(v[2], v[3]);
    }
    u32x4 o = jx ? u32x4{pk[1][0], pk[1][1], pk[0][0], pk[0][1]} : u32x4{pk[0][0], pk[0][1], pk[1][0], pk[1][1]};
    st16<WTM & 1>(ws + WS_QF, 4 * MB, (((tm * 8 + wm * 2 + ma) * 32 + head) * 64 + lane) * 16, o);
  }
  {
    f32x4 sa[2][2];
#pragma unroll
    for (int m = 0; m < 2; ++m) {
      unsigned ra = lds0 + O_ROW + (wm * 32 + (m ^ mx) * 16 + 4 * g) * 8;
      sa[m][0] = *LDS3(f32x4, ra); sa[m][1] = *LDS3(f32x4, ra + 16);
    }
    bf16x8 fr[4];
#pragma unroll
    for (int j = 2; j < 6; ++j) {
      unsigned ca = lds0 + O_CS + (wn * 96 + (j ^ jx) * 16 + r15) * 4;
      float cs = *LDS3(float, ca) + *LDS3(float, ca + 768);
      u32x4 u;
#pragma unroll
      for (int m = 0; m < 2; ++m) {
        float v[4];
#pragma unroll
        for (int r = 0; r < 4; ++r) {
          float t = (acc[m][j][r] - sa[m][r >> 1][(r & 1) * 2] * cs) * sa[m][r >> 1][(r & 1) * 2 + 1];
          v[r] = j < 4 ? phi(t) : t;
        }
        u[m * 2] = pack_bf16(v[0], v[1]); u[m * 2 + 1] = pack_bf16(v[2], v[3]);
      }
      fr[j - 2] = __builtin_bit_cast(bf16x8, u);
    }
    bf16x8 ones = __builtin_bit_cast(bf16x8, u32x4{ONES2, ONES2, ONES2, ONES2});
    f32x4 zero = {0, 0, 0, 0};
#pragma unroll
    for (int fd = 0; fd < 2; ++fd) {
#pragma unroll
      for (int fe = 0; fe < 2; ++fe) {
        f32x4 kv = mfma16(fr[fd], fr[2 + fe], zero);
        *LDS3(f32x4, lds0 + O_RED + ((wave * 4 + ((fd ^ jx) * 2 + (fe ^ jx))) * 64 + lane) * 16) = kv;
      }
      f32x4 z = mfma16(fr[fd], ones, zero);
      if (r15 == 0) *LDS3(f32x4, lds0 + O_Z + ((wave * 2 + (fd ^ jx)) * 4 + g) * 16) = z;
    }
  }
  if (CSA) {
    f32x4 a[2];
#pragma unroll
    for (int c = 0; c < 4; ++c) {
      float lo = 0, hi = 0;
#pragma unroll
      for (int i = 0; i < 8; ++i) { lo += bf_lo(wv[i][c]); hi += bf_hi(wv[i][c]); }
      a[c >> 1][(c & 1) * 2] = lo; a[c >> 1][(c & 1) * 2 + 1] = hi;
    }
    *LDS3(f32x4, lds0 + O_WP + wave * 2048 + lane * 32) = a[0];
    *LDS3(f32x4, lds0 + O_WP + wave * 2048 + lane * 32 + 16) = a[1];
    asm volatile("" ::"v"(hk.d), "v"(a[0]), "v"(a[1]));
  }
  wait_lgkm0(); bar();
  if (CSA && tid >= 384 && tid < 496) {
    int t = tid - 384;
    f32x4 s = *LDS3(f32x4, lds0 + O_WP + t * 16);
#pragma unroll
    for (int k = 1; k < 8; ++k) s += *LDS3(f32x4, lds0 + O_WP + k * 2048 + t * 16);
    st16<WTM & 1>(ws + WS_CS, 16 * 2 * FF * 4, (((b >> 3) & 15) * 2 * FF + (b >> 7) * FF + (b & 7) * 448 + t * 4) * 4, __builtin_bit_cast(u32x4, s));
  }
  {
    int w2 = tid >> 8, i = tid & 255;
    f32x4 s = *LDS3(f32x4, lds0 + O_RED + (w2 * 256 + i) * 16);
#pragma unroll
    for (int k = 1; k < 4; ++k) s += *LDS3(f32x4, lds0 + O_RED + ((k * 2 + w2) * 256 + i) * 16);
    st16<WTM & 1>(ws + WS_KVP, 2 * MB, (((2 * tn + w2) * 16 + tm) * 256 + i) * 16, __builtin_bit_cast(u32x4, s));
    if (tid < 16) {
      int w3 = tid >> 3, c = tid & 7;
      f32x4 z = *LDS3(f32x4, lds0 + O_Z + (w3 * 8 + c) * 16);
#pragma unroll
      for (int k = 1; k < 4; ++k) z += *LDS3(f32x4, lds0 + O_Z + ((k * 2 + w3) * 8 + c) * 16);
      ((f32x4*)(ws + WS_ZP))[((2 * tn + w3) * 16 + tm) * 8 + c] = z;
    }
  }
  STAMPW(0, 2);
}

template <int NK> using CR = Cfg<128, 64, 64, 6, 4, 1, 2, NK, 0, 0>;
template <class C, int KT> DEVI void c_steps(f32x4 (&acc)[C::MF][C::NF], Frags<C>& cur, Frags<C>& nxt, Loader<C>& L, const RdAddr<C>& R, int wave, unsigned lds0,
                                             unsigned& bcur) {
  if constexpr (KT < C::NK) {
    constexpr int S = C::S, last = KT + S - 1 < C::NK - 1 ? KT + S - 1 : C::NK - 1;
    constexpr int n = KT + 1 < S ? 2 * (S - 2 - KT) + 3 * KT : 3 * (last - (KT + 1));
    XHook<(n > 0 ? n : 0)> hk;
    iter<C, 0xfu, (KT + S < C::NK), (KT + 1 < C::NK), 0, 10>(acc, cur, nxt, L, R, wave, lds0, bcur, hk, KT);
    c_steps<C, KT + 1>(acc, nxt, cur, L, R, wave, lds0, bcur);
  }
}
using CD = Cfg<256, 224, 32, 4, 4, 2, 1, 32, 0, 1>;
DEVI float geglu(float x, float u) {
  constexpr float K1 = -2.f * 0.7978845608028654f * 1.4426950408889634f, K2 = K1 * 0.044715f;
  float e = __builtin_amdgcn_exp2f(x * (K1 + K2 * x * x));
  return x * __builtin_amdgcn_rcpf(1.f + e) * u;
}
struct HookD : PfHook<1> {
  char* xch; unsigned xo; int k2;
  DEVI void tail() const {
    glds4(v, p, l);
  }
  DEVI void mid(int kt) const {
  }
};
using CE = Cfg<128, 128, 64, 4, 2, 2, 2, FF / 128, 0, 0>;
DEVI void handoff(char* ws, int wave, int lane, unsigned mine, unsigned watch) {
  asm volatile("s_waitcnt vmcnt(0)" ::: "memory");
  bar();
  if (wave == 0) {
    if (lane == 0) __hip_atomic_store((gu32*)(ws + WS_FLG + mine), 1u, __ATOMIC_RELAXED, __HIP_MEMORY_SCOPE_AGENT);
    __builtin_amdgcn_s_sleep(CSL0);
    gu32* pf = (gu32*)(ws + WS_FLG + watch);
    for (int spin = 0; spin < (1 << 20); ++spin) {
      if (__builtin_amdgcn_ballot_w64(__hip_atomic_load(pf, __ATOMIC_RELAXED, __HIP_MEMORY_SCOPE_AGENT) != 1u) == 0) break;
      __builtin_amdgcn_s_sleep(1);
    }
  }
  asm volatile("" ::: "memory");
  bar();
}
__global__ __launch_bounds__(512, 2) void kF(const void* __restrict__ w, const void* __restrict__ xres, const void* __restrict__ wg, const void* __restrict__ wu,
                                             const void* __restrict__ wd, void* __restrict__ out, char* __restrict__ ws) {
  using C = CR<DM / 64>;
  static_assert(C::NP == 3 && C::PA == 16 && C::P == 24, "piece layout");
  STAMP(1, 0);
  __shared__ __attribute__((aligned(256))) char sm[C::LDS + 4096];
  unsigned lds0 = (unsigned)(size_t)sm, ldb = lds0 + C::LDS;
  int tid = threadIdx.x, lane = tid & 63, wave = __builtin_amdgcn_readfirstlane(tid >> 6);
  int b = blockIdx.x, xcd = b & 7, idx = b >> 3;
  int g = lane >> 4, r15 = lane & 15;
  int dtm = (xcd >> 2) * 4 + (idx & 3), dtn = (xcd & 3) * 8 + (idx >> 2);
  Loader<CD> LD;
  float cv[8];
  {
  int wk = wave & 1, wm = wave >> 1;
  int tm = (xcd >> 1) * 4 + (idx & 3), tn = (xcd & 1) * 8 + (idx >> 2);
  int mf = tm * 8 + wm * 2 + wk, row = mf * 16 + r15;
  int h = (b & 7) * 4 + ((b >> 3) & 3), rg = b >> 5;
  Loader<C> L;
  f32x4 res[2];
  {
    const f32x4* pp = wave < 4 ? (const f32x4*)(ws + WS_KVP) + h * 4096 + tid : (const f32x4*)(ws + WS_ZP) + h * 128 + lane;
    f32x4 p[16];
    if (wave < 4) {
#pragma unroll
      for (int i = 0; i < 16; ++i) p[i] = gload4(pp + i * 256);
    } else {
      p[0] = gload4(pp); p[1] = gload4(pp + 64);
#pragma unroll
      for (int i = 2; i < 16; ++i) p[i] = f32x4{0, 0, 0, 0};
    }
    f32x4 q[2];
#pragma unroll
    for (int i = 0; i < 2; ++i) q[i] = gload4((const u32x4*)(ws + WS_QF) + ((rg * 16 + wave * 2 + i) * 32 + h) * 64 + lane);
    loader_init<C>(L, lds0, wave, lane, ws + WS_OT, tm * 128, DM, w, w, DM, 0, [&](int lc) { return tn * 64 + lc * 8; }, 0, 4u);
    prologue<C>(L, wave, lds0, 1, 4u);
#pragma unroll
    for (int jp = 0; jp < 2; ++jp) res[jp] = gload4((const char*)xres + ((size_t)row * DM + (tn * 8 + 2 * (jp * 2 + (g & 1)) + (g >> 1)) * 8) * 2);
    asm volatile("s_waitcnt vmcnt(%10)" : "+v"(p[0]), "+v"(p[1]), "+v"(p[2]), "+v"(p[3]), "+v"(p[4]), "+v"(p[5]), "+v"(p[6]), "+v"(p[7]), "+v"(p[8]), "+v"(p[9]) : "n"(C::S + 4) : "memory");
    asm volatile("" : "+v"(p[10]), "+v"(p[11]), "+v"(p[12]), "+v"(p[13]), "+v"(p[14]), "+v"(p[15])::"memory");
    f32x4 s = ((p[0] + p[1]) + (p[2] + p[3])) + ((p[4] + p[5]) + (p[6] + p[7])) + (((p[8] + p[9]) + (p[10] + p[11])) + ((p[12] + p[13]) + (p[14] + p[15])));
    if (wave < 4) {
      int fd = wave >> 1, e = (wave & 1) * 16 + r15;
      int fe = (e >> 2) & 1, i = ((e >> 3) << 2) | (e & 3);
      *LDS3(u32x2, ldb + ((fe * 64 + g * 16 + i) * 2 + fd) * 8) = u32x2{pack_bf16(s[0], s[1]), pack_bf16(s[2], s[3])};
    } else {
#pragma unroll
      for (int c = 0; c < 4; ++c) { s[c] = sum_g(s[c]); s[c] += __shfl_xor(s[c], 8); }
      if (wave == 4 && lane < 8) *LDS3(u32x2, ldb + 2048 + ((lane & 3) * 2 + (lane >> 2)) * 8) = u32x2{pack_bf16(s[0], s[1]), pack_bf16(s[2], s[3])};
    }
    wait_lgkm0(); bar();
    asm volatile("s_waitcnt vmcnt(%2)" : "+v"(q[0]), "+v"(q[1]) : "n"(C::S + 2) : "memory");
    bf16x8 k0 = lds_b128(ldb + lane * 16), k1 = lds_b128(ldb + 1024 + lane * 16), zf = lds_b128(ldb + 2048 + g * 16);
    f32x4 zero = {0, 0, 0, 0};
#pragma unroll
    for (int i = 0; i < 2; ++i) {
      bf16x8 qq = __builtin_bit_cast(bf16x8, q[i]);
      f32x4 n0 = mfma16(k0, qq, zero), n1 = mfma16(k1, qq, zero), dn = mfma16(zf, qq, zero);
      float inv = 1.f / (dn[0] + 1e-6f);
      u32x4 o = {pack_bf16(n0[0] * inv, n0[1] * inv), pack_bf16(n0[2] * inv, n0[3] * inv), pack_bf16(n1[0] * inv, n1[1] * inv), pack_bf16(n1[2] * inv, n1[3] * inv)};
      st16<true>(ws + WS_OT, 4 * MB, (((rg * 16 + wave * 2 + i) * 128 + h * 4) * 16 + lane) * 16, o);
    }
  }
  RdAddr<C> R; rd_init<C>(R, lds0, wm, 0, wk, lane, wk, 0, 0);
  STAMP(1, 1);
  handoff(ws, wave, lane, rg * 256 + (b & 31) * 4, (tm >> 1) * 256 + (lane & 31) * 4);
  STAMP(1, 2); STAMP(2, 0);
  prologue<C>(L, wave, lds0, 0, 3u);
  f32x4 acc[2][4];
#pragma unroll
  for (int m = 0; m < 2; ++m)
#pragma unroll
    for (int j = 0; j < 4; ++j) acc[m][j] = f32x4{0, 0, 0, 0};
  {
    Frags<C> f0, f1;
    wait_vm<2 * (C::S - 1)>();
    bar();
    read_all<C>(f0, R, 0);
    unsigned bcur = 0;
    c_steps<C, 0>(acc, f0, f1, L, R, wave, lds0, bcur);
  }
  asm volatile("" : "+v"(res[0]), "+v"(res[1]));
  STAMP(2, 1);
  loader_init<CD>(LD, lds0, wave, lane, ws + WS_HT, dtm * 256, DM, wg, wu, FF, 0, [&](int lc) { return dtn * 112 + lc * 8; }, 0, 12u);
  prologue<CD>(LD, wave, lds0, 1, 12u);
  {
    int ct = tid < 448 ? tid >> 1 : 223;
    const float* cp = (const float*)(ws + WS_CS) + (tid & 1) * 16 * FF + (ct < 112 ? dtn * 112 + ct : FF + dtn * 112 + ct - 112);
#pragma unroll
    for (int i = 0; i < 8; ++i) cv[i] = gload1(cp + i * 2 * FF);
  }
  unsigned scr = lds0 + (wave >> 2) * CD::STAGE + (wave & 3) * 4096 + lane * 16;
#pragma unroll
  for (int j = 0; j < 4; ++j) *LDS3(f32x4, scr + j * 1024) = acc[1][j];
  wait_lgkm0(); bar();
#pragma unroll
  for (int j = 0; j < 4; ++j) acc[0][j] += *LDS3(f32x4, (scr + (wk ? -4096 : 4096)) + j * 1024);
  float ss = 0, sq = 0;
#pragma unroll
  for (int jp = 0; jp < 2; ++jp) {
    f32x4 a = acc[0][2 * jp], c = acc[0][2 * jp + 1];
    u32x4 rr = __builtin_bit_cast(u32x4, res[jp]);
#pragma unroll
    for (int r = 0; r < 4; ++r) { float u = a[r], v = c[r]; swap16(u, v); a[r] = u; c[r] = v; }
    u32x4 o;
    o[0] = pack_bf16(a[0] + bf_lo(rr[0]), a[1] + bf_hi(rr[0]));
    o[1] = pack_bf16(a[2] + bf_lo(rr[1]), a[3] + bf_hi(rr[1]));
    o[2] = pack_bf16(c[0] + bf_lo(rr[2]), c[1] + bf_hi(rr[2]));
    o[3] = pack_bf16(c[2] + bf_lo(rr[3]), c[3] + bf_hi(rr[3]));
    int c8 = tn * 8 + 2 * (jp * 2 + (g & 1)) + (g >> 1);
    st16<true>(ws + WS_HT, 4 * MB, ((mf * 128 + c8) * 16 + r15) * 16, o);
#pragma unroll
    for (int i = 0; i < 4; ++i) { ss = dot2(o[i], ONES2, ss); sq = dot2(o[i], o[i], sq); }
  }
  ss = sum_g(ss); sq = sum_g(sq);
  if (g == 0) __builtin_amdgcn_raw_buffer_store_b64(u32x2{__float_as_uint(ss), __float_as_uint(sq)}, __builtin_amdgcn_make_buffer_rsrc(ws + WS_ST, 0, SEQ * 128, 0x00020000), (row * 16 + tn) * 8, 0, 16);
  STAMPW(2, 2);
  handoff(ws, wave, lane, 2048 + (tm >> 1) * 256 + ((tm & 1) * 16 + tn) * 4, 2048 + dtm * 256 + (lane & 31) * 4);
  }

  int kh = xcd & 1, etm = (xcd >> 1) * 4 + (idx & 3), etn = idx >> 2;
  Loader<CE> LE;
  {
    using C = CD;
    STAMP(3, 0);
    asm volatile("" : "+v"(tid)); lane = tid & 63; g = lane >> 4; r15 = lane & 15;
    constexpr unsigned O_ROW = CR<16>::LDS, O_CS = O_ROW + 2048;
    int wn = wave & 1, wm = wave >> 1, tm = dtm, tn = dtn;
    Loader<C>& L = LD;
    issue_piece<C>(L, 0, wave, lds0); issue_piece<C>(L, 1, wave, lds0);
    const char* sp = ws + WS_ST + (size_t)(tm * 256 + wave * 32) * 128 + lane * 16;
    f32x4 sv[4];
#pragma unroll
    for (int i = 0; i < 4; ++i) sv[i] = gload4c(sp + i * 1024);
    int ct = tid < 448 ? tid >> 1 : 223;
    prologue<C>(L, wave, lds0, 1, 3u);
    wait_vm<2 * (C::S - 1)>();
    asm volatile("" : "+v"(sv[0]), "+v"(sv[1]), "+v"(sv[2]), "+v"(sv[3]), "+v"(cv[0]), "+v"(cv[1]), "+v"(cv[2]), "+v"(cv[3]), "+v"(cv[4]), "+v"(cv[5]), "+v"(cv[6]), "+v"(cv[7]));
    {
      float s[4], q[4];
#pragma unroll
      for (int i = 0; i < 4; ++i) { s[i] = sv[i][0] + sv[i][2]; q[i] = sv[i][1] + sv[i][3]; }
      bool o1 = lane & 1, o2 = lane & 2;
      float sa = (o1 ? s[1] : s[0]) + __shfl_xor(o1 ? s[0] : s[1], 1), sb = (o1 ? s[3] : s[2]) + __shfl_xor(o1 ? s[2] : s[3], 1);
      float qa = (o1 ? q[1] : q[0]) + __shfl_xor(o1 ? q[0] : q[1], 1), qb = (o1 ? q[3] : q[2]) + __shfl_xor(o1 ? q[2] : q[3], 1);
      float ss = (o2 ? sb : sa) + __shfl_xor(o2 ? sa : sb, 2), qq = (o2 ? qb : qa) + __shfl_xor(o2 ? qa : qb, 2);
      ss += __shfl_xor(ss, 4); qq += __shfl_xor(qq, 4);
      float mean = ss * (1.f / DM), var = fmaxf(qq * (1.f / DM) - mean * mean, 0.f);
      float rstd = __builtin_amdgcn_rsqf(var + 1e-5f);
      if (!(lane & 4)) *LDS3(f32x2, lds0 + O_ROW + (wave * 32 + (lane & 3) * 8 + (lane >> 3)) * 8) = f32x2{mean, rstd};
    }
    float cs = ((cv[0] + cv[1]) + (cv[2] + cv[3])) + ((cv[4] + cv[5]) + (cv[6] + cv[7]));
    cs += __shfl_xor(cs, 1);
    if (tid < 448 && !(tid & 1)) *LDS3(float, lds0 + O_CS + ct * 4) = cs;
    RdAddr<C> R; rd_init<C>(R, lds0, wm, wn, 0, lane, 0, 0, 0);
    f32x4 acc[4][7];
#pragma unroll
    for (int m = 0; m < 4; ++m)
#pragma unroll
      for (int j = 0; j < 7; ++j) acc[m][j] = f32x4{0, 0, 0, 0};
    HookD hk; hk.xch = ws + WS_XCH; hk.xo = ((b * 8 + wave) * 256 + lane) * 16; hk.k2 = 4 + (wave & (FST - 1));
    hk.v = lane * 64; hk.l = lds0 + O_ROW + 3072; hk.p = uni_ptr((const char*)wd + (size_t)b * 28672 + (wave < 7 ? wave : 6) * 4096);
    hk.k1 = wave < 7 ? 4 + 3 * wave : -1;
#pragma unroll
    for (int i = 0; i < 4; ++i) st16<FWT>(ws + WS_XCH, 8 * MB, (((b * 8 + wave) * 4 + i) * 64 + lane) * 16, u32x4{~0u, ~0u, ~0u, ~0u});
    mainloop<C, 0x7fu, HookD, true>(acc, L, R, wave, lds0, hk);
    asm volatile("" : "+v"(tid)); lane = tid & 63; g = lane >> 4; r15 = lane & 15;
    STAMP(3, 1);
    loader_init<CE>(LE, lds0, wave, lane, ws + WS_ACT, etm * 128, FF, wd, wd, DM, kh * (FF / 2), [&](int lc) { return etn * 128 + lc * 8; }, 2 * kh, 12u);
    prologue<CE>(LE, wave, lds0, 1, 12u);
    int hi = g >> 1;
    f32x2 st[2];
#pragma unroll
    for (int p = 0; p < 2; ++p) st[p] = *LDS3(f32x2, lds0 + O_ROW + (wm * 64 + (p * 2 + hi) * 16 + r15) * 8);
    int smf = tm * 16 + wm * 4 + ((g & 1) * 2 + hi);
    u32x4* dst = (u32x4*)(ws + WS_ACT) + ((size_t)smf * (FF / 8) + tn * 14 + wn * 7) * 16 + r15;
    const __amdgpu_buffer_rsrc_t drs = __builtin_amdgcn_make_buffer_rsrc(ws + WS_ACT, 0, SEQ * FF * 2, 0x00020000);
    unsigned doff = ((smf * (FF / 8) + tn * 14 + wn * 7) * 16 + r15) * 16;
#pragma unroll
    for (int j = 0; j < 7; ++j) {
      unsigned ca = lds0 + O_CS + ((wn * 7 + j) * 8 + (g & 1) * 4) * 4;
      f32x4 cg = *LDS3(f32x4, ca), cu = *LDS3(f32x4, ca + 448);
      unsigned pk[2][2];
#pragma unroll
      for (int p = 0; p < 2; ++p) {
        float a[4];
#pragma unroll
        for (int r = 0; r < 4; ++r) {
          float x = acc[2 * p][j][r], y = acc[2 * p + 1][j][r];
          swap32(x, y);
          a[r] = geglu((x - st[p][0] * cg[r]) * st[p][1], (y - st[p][0] * cu[r]) * st[p][1]);
        }
        pk[p][0] = pack_bf16(a[0], a[1]); pk[p][1] = pack_bf16(a[2], a[3]);
      }
      swap16u(pk[0][0], pk[1][0]); swap16u(pk[0][1], pk[1][1]);
      __builtin_amdgcn_raw_buffer_store_b128(u32x4{pk[0][0], pk[0][1], pk[1][0], pk[1][1]}, drs, doff + j * 256, 0, DST);
    }
  }
  STAMPW(3, 2);
  handoff(ws, wave, lane, 4096 + dtm * 256 + dtn * 4, 4096 + (lane < 16 ? (etm >> 1) * 256 + (kh * 16 + lane) * 4 : dtm * 256 + (dtn ^ 8) * 4));

  {
  using C = CE;
  STAMP(4, 0);
  asm volatile("" : "+v"(tid)); lane = tid & 63; g = lane >> 4; r15 = lane & 15;
  Loader<C>& L = LE;
  prologue<C>(L, wave, lds0, 0, 3u);
  int wk = wave & 1, wn = (wave >> 1) & 1, wm = wave >> 2, tm = etm, tn = etn;
  int c8 = tn * 16 + wn * 8 + 2 * (2 * wk + (g & 1)) + (g >> 1);
  int mf0 = tm * 8 + wm * 4 + 2 * kh;
  RdAddr<C> R; rd_init<C>(R, lds0, wm, wn, wk, lane, 0, 0, 2 * wk);
  f32x4 acc[4][4];
#pragma unroll
  for (int m = 0; m < 4; ++m)
#pragma unroll
    for (int j = 0; j < 4; ++j) acc[m][j] = f32x4{0, 0, 0, 0};
  NoHook hk;
  mainloop<C, 0xfu, NoHook, true>(acc, L, R, wave, lds0, hk);
  STAMP(4, 1);
  u32x4 res[2];
#pragma unroll
  for (int m = 0; m < 2; ++m) res[m] = __builtin_amdgcn_raw_buffer_load_b128(__builtin_amdgcn_make_buffer_rsrc(ws + WS_HT, 0, 4 * MB, 0x00020000), (((mf0 + m) * 128 + c8) * 16 + r15) * 16, 0, 16);
#pragma unroll
  for (int m = 3; m >= 0; --m)
#pragma unroll
    for (int j = 0; j < 2; ++j) *LDS3(f32x4, lds0 + ((wave * 8 + m * 2 + j) * 64 + lane) * 16) = acc[m][2 + j];
  wait_lgkm0(); bar();
  const __amdgpu_buffer_rsrc_t rs = __builtin_amdgcn_make_buffer_rsrc(ws + WS_XCH, 0, 256 * 8 * 4096, 0x00020000);
#pragma unroll
  for (int m = 3; m >= 0; --m)
#pragma unroll
    for (int j = 0; j < 2; ++j) {
      acc[m][j] += *LDS3(f32x4, lds0 + (((wave ^ 1) * 8 + m * 2 + j) * 64 + lane) * 16);
      if (m >= 2) {
        u32x4 u = __builtin_bit_cast(u32x4, acc[m][j]);
#pragma unroll
        for (int r = 0; r < 4; ++r) u[r] = min(u[r], 0xfffffffeu);
        __builtin_amdgcn_raw_buffer_store_b128(u, rs, (((b * 8 + wave) * 4 + (m - 2) * 2 + j) * 64 + lane) * 16, 0, 16);
      }
    }
  u32x4 x[4];
  if (ESL0) __builtin_amdgcn_s_sleep(ESL0);
  for (int spin = 0; spin < (1 << 20); ++spin) {
    asm volatile("" ::: "memory");
#pragma unroll
    for (int i = 0; i < 4; ++i) x[i] = __builtin_amdgcn_raw_buffer_load_b128(rs, ((((b ^ 1) * 8 + wave) * 4 + i) * 64 + lane) * 16, 0, 16);
    unsigned mx = 0;
#pragma unroll
    for (int i = 0; i < 4; ++i) mx = max(max(mx, max(x[i][0], x[i][1])), max(x[i][2], x[i][3]));
    if (__builtin_amdgcn_ballot_w64(mx == 0xffffffffu) == 0) break;
    if (ESL) __builtin_amdgcn_s_sleep(ESL);
  }
#pragma unroll
  for (int m = 0; m < 2; ++m) {
    f32x4 a = acc[m][0] + __builtin_bit_cast(f32x4, x[2 * m]), c = acc[m][1] + __builtin_bit_cast(f32x4, x[2 * m + 1]);
#pragma unroll
    for (int r = 0; r < 4; ++r) { float u = a[r], v = c[r]; swap16(u, v); a[r] = u; c[r] = v; }
    u32x4 o;
    o[0] = pack_bf16(a[0] + bf_lo(res[m][0]), a[1] + bf_hi(res[m][0]));
    o[1] = pack_bf16(a[2] + bf_lo(res[m][1]), a[3] + bf_hi(res[m][1]));
    o[2] = pack_bf16(c[0] + bf_lo(res[m][2]), c[1] + bf_hi(res[m][2]));
    o[3] = pack_bf16(c[2] + bf_lo(res[m][3]), c[3] + bf_hi(res[m][3]));
    st16<(WTM & 8) != 0>(out, 4 * MB, (((mf0 + m) * 16 + r15) * DM + c8 * 8) * 2, o);
  }
  STAMPW(4, 2);
  }
}

extern "C" void kernel_launch(void* const* d_in, const int* in_sizes, int n_in, void* d_out, int out_size, void* d_ws, size_t ws_size,
                              hipStream_t stream) {
  char* ws = (char*)d_ws;
  kA<<<256, 512, 0, stream>>>(d_in[0], d_in[1], d_in[2], d_in[4], d_in[5], d_in[6], ws);
  kF<<<256, 512, 0, stream>>>(d_in[2], d_in[0], d_in[4], d_in[5], d_in[6], d_out, ws);
}
```

```cpp
#include <hip/hip_runtime.h>
#include <stdint.h>
#define STAGE_D 0
#define ASC 0
#define UMAP 1
#define STAG 2

typedef __attribute__((ext_vector_type(4))) short s16x4;
typedef __attribute__((ext_vector_type(8))) short s16x8;
typedef __attribute__((ext_vector_type(8))) __bf16 bf16x8;
typedef __attribute__((ext_vector_type(2))) __bf16 bf16x2;
typedef __attribute__((ext_vector_type(4))) float f32x4;
typedef __attribute__((ext_vector_type(2))) float f32x2;
typedef __attribute__((ext_vector_type(4))) unsigned u32x4;
typedef __attribute__((ext_vector_type(2))) unsigned u32x2;
#define LDS3(T, a) ((__attribute__((address_space(3))) T*)(size_t)(a))
#define DEVI __device__ __forceinline__

template <int N> DEVI void wait_vm() { asm volatile("s_waitcnt vmcnt(%0)" ::"n"(N) : "memory"); }
DEVI void wait_lgkm0() { asm volatile("s_waitcnt lgkmcnt(0)" ::: "memory"); }
DEVI void bar() { __builtin_amdgcn_s_barrier(); }
DEVI void glds16(unsigned voff, const char* sbase, unsigned lds) {
  asm volatile("s_mov_b32 m0, %2\n\ts_nop 0\n\tglobal_load_lds_dwordx4 %0, %1" ::"v"(voff), "s"(sbase), "s"(lds) : "memory");
}
DEVI void glds16c(unsigned voff, const char* sbase, unsigned lds) {
  asm volatile("s_mov_b32 m0, %2\n\ts_nop 0\n\tglobal_load_lds_dwordx4 %0, %1 sc1" ::"v"(voff), "s"(sbase), "s"(lds) : "memory");
}
DEVI bf16x8 lds_b128(unsigned a) { return *LDS3(bf16x8, a); }
DEVI s16x4 lds_tr(unsigned a) { return __builtin_amdgcn_ds_read_tr16_b64_v4i16(LDS3(s16x4, a)); }
DEVI unsigned pack_bf16(float lo, float hi) {
  bf16x2 v; v[0] = (__bf16)lo; v[1] = (__bf16)hi; return __builtin_bit_cast(unsigned, v);
}
DEVI float bf_lo(unsigned u) { return __uint_as_float(u << 16); }
DEVI float bf_hi(unsigned u) { return __uint_as_float(u & 0xffff0000u); }
DEVI float dot2(unsigned a, unsigned b, float c) {
  return __builtin_amdgcn_fdot2_f32_bf16(__builtin_bit_cast(bf16x2, a), __builtin_bit_cast(bf16x2, b), c, false);
}
DEVI void swap16(float& a, float& b) {
  auto r = __builtin_amdgcn_permlane16_swap(__float_as_uint(a), __float_as_uint(b), false, false);
  a = __uint_as_float(r[0]); b = __uint_as_float(r[1]);
}
DEVI void swap16u(unsigned& a, unsigned& b) {
  auto r = __builtin_amdgcn_permlane16_swap(a, b, false, false);
  a = r[0]; b = r[1];
}
DEVI void swap32(float& a, float& b) {
  auto r = __builtin_amdgcn_permlane32_swap(__float_as_uint(a), __float_as_uint(b), false, false);
  a = __uint_as_float(r[0]); b = __uint_as_float(r[1]);
}
DEVI float sum_g(float x) {
  float a = x, b = x; swap16(a, b); x = a + b;
  a = x; b = x; swap32(a, b); return a + b;
}
DEVI f32x4 mfma16(bf16x8 a, bf16x8 b, f32x4 c) { return __builtin_amdgcn_mfma_f32_16x16x32_bf16(a, b, c, 0, 0, 0); }

template <int BM_, int BN_, int BK_, int S_, int WM_, int WN_, int WK_, int NK_, int AMODE_, int BMODE_, int NPF_ = 0>
struct Cfg {
  static constexpr int NPF = NPF_;
  static constexpr int BM = BM_, BN = BN_, BK = BK_, S = S_, WM = WM_, WN = WN_, WK = WK_, NK = NK_, AMODE = AMODE_, BMODE = BMODE_;
  static constexpr int NW = WM * WN * WK;
  static constexpr int MF = BM / WM / 16, NF = BN / WN / 16, KF = BK / 32, KFW = KF / WK;
  static constexpr int A_BYTES = BM * BK * 2;
  static constexpr int B_BYTES = BN * BK * 2 + (BMODE == 1 ? 256 : 0);
  static constexpr int STAGE = A_BYTES + B_BYTES;
  static constexpr int PA = A_BYTES / 1024, PB = BN * BK * 2 / 1024, P = PA + PB;
  static constexpr int NLW = NPF ? NW - 1 : NW;
  static constexpr int NP = (P + NLW - 1) / NLW;
  static constexpr int FULLW = P - (NP - 1) * NLW;
  static constexpr int RS = (BMODE == 1 ? BN : BN * 2);
  static constexpr int LDS = S * STAGE;
  static constexpr bool SLOT = (MF == 2);
};

template <class C> struct Frags { bf16x8 a[C::KFW][C::MF]; bf16x8 b[C::KFW][C::NF]; };
template <class C> struct Loader {
  const char* sbase[C::NP]; unsigned voff[C::NP]; unsigned ldso[C::NP]; unsigned sstr[C::NP];
  const char* pfb[4]; unsigned pfv, pfl, pfs[4];
};
DEVI void glds4(unsigned voff, const char* sbase, unsigned lds) {
  asm volatile("s_mov_b32 m0, %2\n\ts_nop 0\n\tglobal_load_lds_dword %0, %1" ::"v"(voff), "s"(sbase), "s"(lds) : "memory");
}
template <class C> DEVI void issue_pf(Loader<C>& L, int i) { glds4(L.pfv, L.pfb[i], L.pfl); L.pfb[i] += L.pfs[i]; }
DEVI const char* uni_ptr(const char* p) {
  unsigned long long u = (unsigned long long)p;
  unsigned lo = __builtin_amdgcn_readfirstlane((unsigned)u), hi = __builtin_amdgcn_readfirstlane((unsigned)(u >> 32));
  return (const char*)(((unsigned long long)hi << 32) | lo);
}

template <class C> DEVI void issue_piece(Loader<C>& L, int i, int wave, unsigned bufbase) {
  if (C::AMODE == 0 && i < 2) glds16c(L.voff[i], L.sbase[i], bufbase + L.ldso[i]);
  else if (i < C::NP - 1 ? (C::NLW == C::NW || wave < C::NLW) : (C::FULLW == C::NW || wave < C::FULLW)) glds16(L.voff[i], L.sbase[i], bufbase + L.ldso[i]);
  L.sbase[i] += L.sstr[i];
}
template <class C, class ColMap>
DEVI void loader_init(Loader<C>& L, unsigned lds0, int wave, int lane, const void* A, int m0, int lda, const void* B0, const void* B1, int ldb, int k0,
                      ColMap colmap, int mxor = 0, unsigned pmask = ~0u) {
#pragma unroll
  for (int i = 0; i < C::NP; ++i) {
    int p = wave + i * C::NLW;
    if (p >= C::P) p = C::P - 1;
    if (p < C::PA) {
      if (C::AMODE == 0) {
        int mb = p / C::KF, kb = p % C::KF;
        L.sbase[i] = (const char*)A + ((size_t)(m0 / 16 + (mb ^ mxor)) * (lda / 8) + (k0 / 8 + kb * 4)) * 256;
        L.voff[i] = lane * 16;
        L.sstr[i] = C::BK / 8 * 256;
      } else {
        int row = 8 * p + (lane >> 3), pos = lane & 7, lc = pos ^ ((row >> 1) & 7);
        L.sbase[i] = (const char*)A + ((size_t)(m0 + 8 * p) * lda + k0) * 2;
        L.voff[i] = ((lane >> 3) * lda + lc * 8) * 2;
        L.sstr[i] = C::BK * 2;
      }
      L.ldso[i] = p * 1024;
    } else {
      int pb = p - C::PA;
      if (C::BMODE == 0) {
        constexpr int CH = C::BN / 8;
        int ci = pb * 64 + lane, k = ci / CH, c = ci % CH, u = c >> 1;
        int sg = (C::RS % 256 == 0) ? ((k & 3) | (((k >> 3) & 1) << 2)) : (C::RS % 256 == 128) ? (((k >> 1) & 1) | (((k >> 3) & 1) << 1)) : ((k >> 3) & 1);
        int lc = ((u ^ sg) << 1) | (c & 1);
        L.sbase[i] = (const char*)B0 + (size_t)k0 * ldb * 2;
        L.voff[i] = (k * ldb + colmap(lc)) * 2;
        L.ldso[i] = C::A_BYTES + pb * 1024;
      } else {
        constexpr int CH = C::BN / 16, HP = C::PB / 2;
        int half = pb / HP, pp = pb % HP;
        int ci = pp * 64 + lane, k = ci / CH, c = ci % CH, lc = c ^ ((k >> 3) & 1);
        L.sbase[i] = (const char*)(half ? B1 : B0) + (size_t)k0 * ldb * 2;
        L.voff[i] = (k * ldb + colmap(lc)) * 2;
        L.ldso[i] = C::A_BYTES + half * (HP * 1024 + 128) + pp * 1024;
      }
      L.sstr[i] = C::BK * ldb * 2;
    }
    {
      unsigned long long u = (unsigned long long)L.sbase[i];
      unsigned lo = __builtin_amdgcn_readfirstlane((unsigned)u), hi = __builtin_amdgcn_readfirstlane((unsigned)(u >> 32));
      L.sbase[i] = (const char*)(((unsigned long long)hi << 32) | lo);
      L.ldso[i] = __builtin_amdgcn_readfirstlane(L.ldso[i]);
      L.sstr[i] = __builtin_amdgcn_readfirstlane(L.sstr[i]);
    }
    if ((pmask >> i) & 1) issue_piece<C>(L, i, wave, lds0);
  }
}

template <class C, int NST, int X = 0> DEVI void wait_stages(int wave) {
  if (C::FULLW == C::NW || wave < C::FULLW) wait_vm<NST * C::NP + X>();
  else if (C::NLW == C::NW || wave < C::NLW) wait_vm<NST*(C::NP - 1) + X>();
}

template <class C> struct RdAddr { unsigned a[C::KFW][2]; unsigned b[C::NF]; unsigned kbo[2]; };
template <class C> DEVI void rd_init(RdAddr<C>& R, unsigned lds0, int wm, int wn, int wk, int lane, int mx, int kx, int jx) {
  int g = lane >> 4, r15 = lane & 15, q = (lane & 15) >> 2, p = lane & 3;
#pragma unroll
  for (int kb = 0; kb < C::KFW; ++kb)
#pragma unroll
    for (int m = 0; m < 2; ++m) {
      int ka = wk * C::KFW + (kb ^ kx), ma = wm * C::MF + (m ^ mx);
      if (C::AMODE == 0) R.a[kb][m] = lds0 + (ma * C::KF + ka) * 1024 + lane * 16;
      else R.a[kb][m] = lds0 + (ma * 16 + r15) * 128 + (((ka * 4 + g) ^ ((r15 >> 1) & 7)) * 16);
    }
  R.kbo[0] = kx * 32 * C::RS; R.kbo[1] = (1 ^ kx) * 32 * C::RS;
  int krow = wk * C::KFW * 32 + 8 * g + q;
#pragma unroll
  for (int j = 0; j < C::NF; ++j) {
    int U = wn * C::NF + (j ^ jx);
    if (UMAP && C::AMODE == 1) U = (j >> 1) * 4 + wn * 2 + ((j ^ jx) & 1);
    if (C::BMODE == 0) {
      int sg = (C::RS % 256 == 0) ? (q | ((g & 1) << 2)) : (C::RS % 256 == 128) ? ((q >> 1) | ((g & 1) << 1)) : (g & 1);
      R.b[j] = lds0 + C::A_BYTES + krow * C::RS + ((U ^ sg) * 32) + p * 8;
    } else {
      R.b[j] = lds0 + C::A_BYTES + (p >> 1) * (C::PB / 2 * 1024 + 128) + krow * C::RS + ((U ^ (g & 1)) * 16) + (p & 1) * 8;
    }
  }
}
template <class C> DEVI void read_a(Frags<C>& f, const RdAddr<C>& R, unsigned so, int kb, int mf) {
  if (C::SLOT) f.a[kb][mf] = lds_b128(R.a[kb][mf & 1] + so);
  else f.a[kb][mf] = lds_b128(R.a[0][0] + so + (mf * C::KF + kb) * 1024);
}
template <class C> DEVI void read_b(Frags<C>& f, const RdAddr<C>& R, unsigned so, int kb, int j) {
  unsigned a = (C::AMODE == 1) ? R.b[j] + (so + R.kbo[kb & 1]) : R.b[j] + so + (kb * 32) * C::RS;
  s16x4 lo = lds_tr(a);
  s16x4 hi = lds_tr(a + 4 * C::RS);
  s16x8 v = __builtin_shufflevector(lo, hi, 0, 1, 2, 3, 4, 5, 6, 7);
  f.b[kb][j] = __builtin_bit_cast(bf16x8, v);
}
template <class C> DEVI void read_all(Frags<C>& f, const RdAddr<C>& R, unsigned so) {
#pragma unroll
  for (int kb = 0; kb < C::KFW; ++kb) {
#pragma unroll
    for (int m = 0; m < C::MF; ++m) read_a<C>(f, R, so, kb, m);
#pragma unroll
    for (int j = 0; j < C::NF; ++j) read_b<C>(f, R, so, kb, j);
  }
}

template <class C, unsigned SWAPMASK, bool ISSUE, bool READ, int NOUT, int PF, class Hook>
DEVI void iter(f32x4 (&acc)[C::MF][C::NF], const Frags<C>& cur, Frags<C>& nxt, Loader<C>& L, const RdAddr<C>& R, int wave, unsigned lds0,
               unsigned& bcur, Hook& hook, int kt) {
  wait_lgkm0();
  if (READ) { wait_stages<C, NOUT, (PF == 10 ? Hook::NT : 0)>(wave); }
  bar();
  if (PF == 9) hook.tail();
  unsigned bn = bcur + C::STAGE; if (bn == C::LDS) bn = 0;
  constexpr int NM = C::KFW * C::MF * C::NF;
  constexpr int NR = C::KFW * (C::MF + C::NF);
  constexpr int SEG = C::NP + 1;
#pragma unroll
  for (int s = 0; s < SEG; ++s) {
    if (READ) {
#pragma unroll
      for (int r = s * NR / SEG; r < (s + 1) * NR / SEG; ++r) {
        int kb = r / (C::MF + C::NF), x = r % (C::MF + C::NF);
        if (x < C::MF) read_a<C>(nxt, R, bn, kb, x); else read_b<C>(nxt, R, bn, kb, x - C::MF);
      }
    }
#pragma unroll
    for (int i = s * NM / SEG; i < (s + 1) * NM / SEG; ++i) {
      int kb = i / (C::MF * C::NF), x = i % (C::MF * C::NF), j = x / C::MF, m = x % C::MF;
      if ((SWAPMASK >> j) & 1) acc[m][j] = mfma16(cur.b[kb][j], cur.a[kb][m], acc[m][j]);
      else acc[m][j] = mfma16(cur.a[kb][m], cur.b[kb][j], acc[m][j]);
    }
    hook.seg(cur, s, SEG);
#pragma unroll
    for (int x = 0; x < 8; ++x) {
      __builtin_amdgcn_sched_group_barrier(0x8, 1, 0);
      __builtin_amdgcn_sched_group_barrier(0x100, 1, 0);
      if (Hook::VALU) __builtin_amdgcn_sched_group_barrier(0x2, Hook::VALU, 0);
    }
    __builtin_amdgcn_sched_barrier(0);
    if (PF && PF < 9 && (s == 1 || s == 3) && (PF - 1) * 2 + (s >> 1) < C::NPF) { if (wave == C::NW - 1) issue_pf<C>(L, (PF - 1) * 2 + (s >> 1)); }
    if (ISSUE && s < C::NP) { issue_piece<C>(L, s, wave, lds0 + bcur); __builtin_amdgcn_sched_barrier(0); }
    if (s == SEG - 1) hook.mid(kt);
  }
  bcur = bn;
}

template <class C> DEVI void prologue(Loader<C>& L, int wave, unsigned lds0, int s0 = 1, unsigned pmask = ~0u) {
#pragma unroll
  for (int s = s0; s < C::S; ++s) {
#pragma unroll
    for (int i = 0; i < C::NP; ++i) if ((pmask >> i) & 1) issue_piece<C>(L, i, wave, lds0 + s * C::STAGE);
  }
}

struct NoHook { static constexpr int VALU = 0, NT = 0; template <class F> DEVI void seg(const F&, int, int) const {} DEVI void tail() const {} DEVI void mid(int) const {} };
template <int N> struct XHook : NoHook { static constexpr int NT = N; };
template <class C, unsigned SWAPMASK, class Hook, bool PEEL = false>
DEVI void mainloop(f32x4 (&acc)[C::MF][C::NF], Loader<C>& L, const RdAddr<C>& R, int wave, unsigned lds0, Hook& hook) {
  constexpr int S = C::S, NK = C::NK;
  Frags<C> f0, f1;
  if (PEEL) wait_vm<2 * (S - 1)>(); else wait_stages<C, S - 1>(wave);
  bar();
  read_all<C>(f0, R, 0);
  unsigned bcur = 0;
  constexpr int NMAIN = NK - S;
  constexpr int NPAIR = NMAIN / 2;
  if constexpr (PEEL) {
    static_assert(S == 4 && C::PA == 16 && C::NW == 8, "piece layout");
    XHook<4> h0; XHook<2> h1;
    iter<C, SWAPMASK, true, true, 0, 10>(acc, f0, f1, L, R, wave, lds0, bcur, h0, 0);
    iter<C, SWAPMASK, true, true, 1, 10>(acc, f1, f0, L, R, wave, lds0, bcur, h1, 1);
  }
  for (int it = PEEL; it < NPAIR; ++it) {
    iter<C, SWAPMASK, true, true, S - 2, (C::NPF > 0 ? 1 : 0)>(acc, f0, f1, L, R, wave, lds0, bcur, hook, 2 * it);
    iter<C, SWAPMASK, true, true, S - 2, (C::NPF > 2 ? 2 : 0)>(acc, f1, f0, L, R, wave, lds0, bcur, hook, 2 * it + 1);
  }
  constexpr int T0 = 2 * NPAIR;
#define TAILIT(T, CUR, NXT)                                                        \
  if constexpr (T0 + (T) < NK) {                                                   \
    constexpr int kt = T0 + (T);                                                   \
    constexpr bool iss = kt + S < NK, rd = kt + 1 < NK;                            \
    constexpr int last_issued = (kt + S - 1 < NK - 1) ? kt + S - 1 : NK - 1;       \
    constexpr int nout = rd ? last_issued - (kt + 1) : 0;                          \
    iter<C, SWAPMASK, iss, rd, nout, (kt == NK - S ? 9 : kt > NK - S ? 10 : 0)>(acc, CUR, NXT, L, R, wave, lds0, bcur, hook, kt); \
  }
  TAILIT(0, f0, f1) TAILIT(1, f1, f0) TAILIT(2, f0, f1) TAILIT(3, f1, f0) TAILIT(4, f0, f1) TAILIT(5, f1, f0) TAILIT(6, f0, f1) TAILIT(7, f1, f0)
#undef TAILIT
}
template <int N> struct PfHook {
  static constexpr int VALU = 0, NT = (STAGE_D ? 0 : N);
  const char* p; unsigned v, l; int k1;
  template <class F> DEVI void seg(const F&, int, int) const {}
  DEVI void tail() const {
#pragma unroll
    for (int i = 0; i < N; ++i) glds4(v, p + i * 4096, l);
  }
  DEVI void mid(int kt) const {
  }
};

#define FILL 1
#define ESL 1
#define ESL0 40
#define FST 4
#define FWT 1
#define FUSE 1
#define CFENCE 1
#define CSL0 8
#define EPF 0
#define TNX 0
#define LINPF 2
#define DST 16
#define CSA 1
#define STAGD 4
#define STAG0 3
constexpr int SEQ = 2048, DM = 1024, FF = 3584;
constexpr size_t MB = 1u << 20;
constexpr size_t WS_QF = 0;
constexpr size_t WS_OT = 4 * MB;
constexpr size_t WS_HT = 8 * MB;
constexpr size_t WS_KVP = 12 * MB;
constexpr size_t WS_ZP = 14 * MB;
constexpr size_t WS_ST = 14 * MB + 256 * 1024;
constexpr size_t WS_CS = 14 * MB + 768 * 1024;
constexpr size_t WS_ACT = 16 * MB;
constexpr size_t WS_FLG = 15 * MB + 512 * 1024;
constexpr size_t WS_XCH = 32 * MB;
#define DIAG3 0
#define STAMP3(kid, pos)
#define STAMP(kid, i)
#define STAMPW(kid, i)
typedef __attribute__((address_space(1))) unsigned gu32;
constexpr unsigned ONES2 = 0x3f803f80u;
#define WTM 15
template <bool WT> DEVI void st16(void* base, unsigned bytes, unsigned off, u32x4 v) {
  if (WT) __builtin_amdgcn_raw_buffer_store_b128(v, __builtin_amdgcn_make_buffer_rsrc(base, 0, bytes, 0x00020000), off, 0, 16);
  else *(u32x4*)((char*)base + off) = v;
}

DEVI f32x4 gload4(const void* p) { f32x4 r; asm volatile("global_load_dwordx4 %0, %1, off" : "=v"(r) : "v"(p) : "memory"); return r; }
DEVI f32x4 gload4c(const void* p) { f32x4 r; asm volatile("global_load_dwordx4 %0, %1, off sc1" : "=v"(r) : "v"(p) : "memory"); return r; }
DEVI float gload1(const void* p) { float r; asm volatile("global_load_dword %0, %1, off" : "=v"(r) : "v"(p) : "memory"); return r; }
DEVI float phi(float t) { return (t > 0.f ? t + 1.f : __expf(t)) + 1e-6f; }

#define SV 0
#define SVV 1
#define SA 3
using CA = Cfg<128, 192, 64, SA, 4, 2, 1, 16, 1, 0, 0>;
DEVI void touch(unsigned& d, unsigned voff, const char* sbase, int a, int b) {
  asm volatile("s_cmp_lg_u32 %3, %4\n\ts_cbranch_scc1 .Ltch%=\n\tglobal_load_dword %0, %1, %2\n.Ltch%=:" : "+v"(d) : "v"(voff), "s"(sbase), "s"(a), "s"(b) : "memory", "scc");
}
struct HookA {
  static constexpr int VALU = SV ? SVV : 0, NT = 1;
  f32x4 rs, rq, c[3];
  const char* p; unsigned v, v2, d; int k1;
  char* xch; unsigned xo;
  DEVI void fill() const {
#pragma unroll
    for (int i = 0; i < 4; ++i) st16<FWT>(xch, 8 * MB, xo + i * 1024, u32x4{~0u, ~0u, ~0u, ~0u});
  }
  DEVI void tail() { touch(d, v2, p, k1, k1); if (FILL == 5) fill(); }
  DEVI void mid(int kt) {
    if (kt < CA::NK - CA::S) touch(d, v, p, kt, k1);
  }
  template <bool V> DEVI void acc1(f32x4& r, bf16x8 x, bool sqr) {
    bf16x8 ones = __builtin_bit_cast(bf16x8, u32x4{ONES2, ONES2, ONES2, ONES2});
    if (V) {
      u32x4 u = __builtin_bit_cast(u32x4, x);
#pragma unroll
      for (int i = 0; i < 4; ++i) r[i & 1] = dot2(u[i], sqr ? u[i] : ONES2, r[i & 1]);
    } else r = mfma16(sqr ? x : ones, x, r);
  }
  template <class F> DEVI void seg(const F& f, int s, int SEG) {
#pragma unroll
    for (int u = s * 7 / SEG; u < (s + 1) * 7 / SEG; ++u) {
      if (u == 0) acc1<(SV & 1) != 0>(rs, f.a[0][0], false);
      if (u == 1) acc1<(SV & 2) != 0>(rq, f.a[0][0], true);
      if (u == 2) acc1<(SV & 4) != 0>(c[0], f.b[0][0], false);
      if (u == 3) acc1<(SV & 4) != 0>(c[1], f.b[0][2], false);
      if (u == 4) acc1<(SV & 4) != 0>(c[2], f.b[0][4], false);
      if (u == 5) acc1<(SV & 1) != 0>(rs, f.a[1][0], false);
      if (u == 6) acc1<(SV & 2) != 0>(rq, f.a[1][0], true);
    }
  }
};

__global__ __launch_bounds__(512, 2) void kA(const void* __restrict__ x, const void* __restrict__ wqkv, const void* __restrict__ wo, const void* __restrict__ wg,
                                             const void* __restrict__ wu, const void* __restrict__ wd, char* __restrict__ ws) {
  using C = CA;
  STAMP(0, 0);
  __shared__ __attribute__((aligned(256))) char sm[C::LDS];
  unsigned lds0 = (unsigned)(size_t)sm;
  int tid = threadIdx.x, lane = tid & 63, wave = __builtin_amdgcn_readfirstlane(tid >> 6);
  int wn = wave & 1, wm = wave >> 1;
  int b = blockIdx.x, xcd = b & 7, idx = b >> 3;
  int tm = (xcd >> 1) * 4 + (idx & 3), tn = (xcd & 1) * 8 + ((idx >> 2) ^ TNX);
  Loader<C> L;
  loader_init<C>(L, lds0, wave, lane, x, tm * 128, DM, wqkv, wqkv, 3 * DM, 0, [&](int lc) {
    return (lc >> 3) * DM + tn * 64 + (lc & 7) * 8;
  });
  STAMP3(0, 1);
  prologue<C>(L, wave, lds0);
  STAMP3(0, 2);
  HookA hk; hk.d = 0; hk.xch = ws + WS_XCH; hk.xo = ((b * 8 + wave) * 256 + lane) * 16;
  touch(hk.d, lane * 64, uni_ptr((const char*)wqkv + (size_t)b * 24576 + (wave < 6 ? wave : 0) * 4096), (wave + 2) >> 3, 0);
  int mx = wn, kx = wm & 1, jx = wm >> 1;
  RdAddr<C> R; rd_init<C>(R, lds0, wm, wn, 0, lane, mx, kx, jx);
  f32x4 acc[2][6];
#pragma unroll
  for (int m = 0; m < 2; ++m)
#pragma unroll
    for (int j = 0; j < 6; ++j) acc[m][j] = f32x4{0, 0, 0, 0};
  const char* wp = uni_ptr((const char*)(b >> 7 ? wu : wg) + (size_t)((b >> 3) & 15) * 64 * FF * 2 + (b & 7) * 896);
  {
    int i1 = wave * 128 + lane, i2 = i1 + 64;
    hk.p = wave < 7 ? wp : uni_ptr((const char*)wo + (size_t)b * 8192);
    hk.v = wave < 7 ? (i1 / 14) * (FF * 2) + (i1 % 14) * 64 : lane * 64;
    hk.v2 = wave < 7 ? (i2 / 14) * (FF * 2) + (i2 % 14) * 64 : lane * 64 + 4096;
    hk.k1 = STAG0 + wave;
  }
  hk.rs = hk.rq = hk.c[0] = hk.c[1] = hk.c[2] = f32x4{0, 0, 0, 0};
  if (FILL == 7) hk.fill();
  mainloop<C, 0x3u>(acc, L, R, wave, lds0, hk);
  STAMP(0, 1);
  if (tid < 96) __hip_atomic_store((gu32*)(ws + WS_FLG + ((tid >> 5) * 256 + b) * 128 + (tid & 31) * 4), 0u, __ATOMIC_RELAXED, __HIP_MEMORY_SCOPE_AGENT);
  u32x4 wv[8];
  if (CSA) {
    int l56 = lane < 56 ? lane : 55;
#pragma unroll
    for (int i = 0; i < 8; ++i) wv[i] = *(const __attribute__((address_space(1))) u32x4*)(size_t)(wp + (wave * 8 + i) * (FF * 2) + l56 * 16);
  }
  int g = lane >> 4, r15 = lane & 15;
  constexpr unsigned O_ROW = 0, O_CS = 1024, O_Z = 4096, O_RED = 8192, O_WP = 40960;
  {
    int rr = r15 & 3;
    float s = hk.rs[0], q = rr == 0 ? hk.rq[0] : rr == 1 ? hk.rq[1] : rr == 2 ? hk.rq[2] : hk.rq[3];
    if (SV & 1) s = sum_g(hk.rs[0] + hk.rs[1]);
    if (SV & 2) q = sum_g(hk.rq[0] + hk.rq[1]);
    if (SV & 4) {
#pragma unroll
      for (int i = 0; i < 3; ++i) hk.c[i][0] = sum_g(hk.c[i][0] + hk.c[i][1]);
    }
    float mean = s * (1.f / DM), var = fmaxf(q * (1.f / DM) - mean * mean, 0.f);
    float rstd = __builtin_amdgcn_rsqf(var + 1e-5f);
    if (g == (r15 >> 2)) *LDS3(f32x2, lds0 + O_ROW + (wm * 32 + wn * 16 + r15) * 8) = f32x2{mean, rstd};
    float cc = g == 1 ? hk.c[0][0] : g == 2 ? hk.c[1][0] : hk.c[2][0];
    if (g) *LDS3(float, lds0 + O_CS + (kx * 192 + wn * 96 + (((g - 1) * 2) ^ jx) * 16 + r15) * 4) = cc;
  }
  wait_lgkm0(); bar();
  int head = 2 * tn + wn;
#pragma unroll
  for (int m = 0; m < 2; ++m) {
    int ma = m ^ mx;
    f32x2 st = *LDS3(f32x2, lds0 + O_ROW + (wm * 32 + ma * 16 + r15) * 8);
    unsigned pk[2][2];
#pragma unroll
    for (int j = 0; j < 2; ++j) {
      unsigned ca = lds0 + O_CS + (wn * 96 + (j ^ jx) * 16 + 4 * g) * 4;
      f32x4 cs = *LDS3(f32x4, ca) + *LDS3(f32x4, ca + 768);
      float v[4];
#pragma unroll
      for (int r = 0; r < 4; ++r) v[r] = phi((acc[m][j][r] - st[0] * cs[r]) * st[1]);
      pk[j][0] = pack_bf16(v[0], v[1]); pk[j][1] = pack_bf16(v[2], v[3]);
    }
    u32x4 o = jx ? u32x4{pk[1][0], pk[1][1], pk[0][0], pk[0][1]} : u32x4{pk[0][0], pk[0][1], pk[1][0], pk[1][1]};
    st16<WTM & 1>(ws + WS_QF, 4 * MB, (((tm * 8 + wm * 2 + ma) * 32 + head) * 64 + lane) * 16, o);
  }
  {
    f32x4 sa[2][2];
#pragma unroll
    for (int m = 0; m < 2; ++m) {
      unsigned ra = lds0 + O_ROW + (wm * 32 + (m ^ mx) * 16 + 4 * g) * 8;
      sa[m][0] = *LDS3(f32x4, ra); sa[m][1] = *LDS3(f32x4, ra + 16);
    }
    bf16x8 fr[4];
#pragma unroll
    for (int j = 2; j < 6; ++j) {
      unsigned ca = lds0 + O_CS + (wn * 96 + (j ^ jx) * 16 + r15) * 4;
      float cs = *LDS3(float, ca) + *LDS3(float, ca + 768);
      u32x4 u;
#pragma unroll
      for (int m = 0; m < 2; ++m) {
        float v[4];
#pragma unroll
        for (int r = 0; r < 4; ++r) {
          float t = (acc[m][j][r] - sa[m][r >> 1][(r & 1) * 2] * cs) * sa[m][r >> 1][(r & 1) * 2 + 1];
          v[r] = j < 4 ? phi(t) : t;
        }
        u[m * 2] = pack_bf16(v[0], v[1]); u[m * 2 + 1] = pack_bf16(v[2], v[3]);
      }
      fr[j - 2] = __builtin_bit_cast(bf16x8, u);
    }
    bf16x8 ones = __builtin_bit_cast(bf16x8, u32x4{ONES2, ONES2, ONES2, ONES2});
    f32x4 zero = {0, 0, 0, 0};
#pragma unroll
    for (int fd = 0; fd < 2; ++fd) {
#pragma unroll
      for (int fe = 0; fe < 2; ++fe) {
        f32x4 kv = mfma16(fr[fd], fr[2 + fe], zero);
        *LDS3(f32x4, lds0 + O_RED + ((wave * 4 + ((fd ^ jx) * 2 + (fe ^ jx))) * 64 + lane) * 16) = kv;
      }
      f32x4 z = mfma16(fr[fd], ones, zero);
      if (r15 == 0) *LDS3(f32x4, lds0 + O_Z + ((wave * 2 + (fd ^ jx)) * 4 + g) * 16) = z;
    }
  }
  if (CSA) {
    f32x4 a[2];
#pragma unroll
    for (int c = 0; c < 4; ++c) {
      float lo = 0, hi = 0;
#pragma unroll
      for (int i = 0; i < 8; ++i) { lo += bf_lo(wv[i][c]); hi += bf_hi(wv[i][c]); }
      a[c >> 1][(c & 1) * 2] = lo; a[c >> 1][(c & 1) * 2 + 1] = hi;
    }
    *LDS3(f32x4, lds0 + O_WP + wave * 2048 + lane * 32) = a[0];
    *LDS3(f32x4, lds0 + O_WP + wave * 2048 + lane * 32 + 16) = a[1];
    asm volatile("" ::"v"(hk.d), "v"(a[0]), "v"(a[1]));
  }
  wait_lgkm0(); bar();
  if (CSA && tid >= 384 && tid < 496) {
    int t = tid - 384;
    f32x4 s = *LDS3(f32x4, lds0 + O_WP + t * 16);
#pragma unroll
    for (int k = 1; k < 8; ++k) s += *LDS3(f32x4, lds0 + O_WP + k * 2048 + t * 16);
    st16<WTM & 1>(ws + WS_CS, 16 * 2 * FF * 4, (((b >> 3) & 15) * 2 * FF + (b >> 7) * FF + (b & 7) * 448 + t * 4) * 4, __builtin_bit_cast(u32x4, s));
  }
  {
    int w2 = tid >> 8, i = tid & 255;
    f32x4 s = *LDS3(f32x4, lds0 + O_RED + (w2 * 256 + i) * 16);
#pragma unroll
    for (int k = 1; k < 4; ++k) s += *LDS3(f32x4, lds0 + O_RED + ((k * 2 + w2) * 256 + i) * 16);
    st16<WTM & 1>(ws + WS_KVP, 2 * MB, (((2 * tn + w2) * 16 + tm) * 256 + i) * 16, __builtin_bit_cast(u32x4, s));
    if (tid < 16) {
      int w3 = tid >> 3, c = tid & 7;
      f32x4 z = *LDS3(f32x4, lds0 + O_Z + (w3 * 8 + c) * 16);
#pragma unroll
      for (int k = 1; k < 4; ++k) z += *LDS3(f32x4, lds0 + O_Z + ((k * 2 + w3) * 8 + c) * 16);
      st16<true>(ws + WS_ZP, 65536, (((2 * tn + w3) * 16 + tm) * 8 + c) * 16, __builtin_bit_cast(u32x4, z));
    }
  }
  STAMPW(0, 2);
}

template <int NK> using CR = Cfg<128, 64, 64, 6, 4, 1, 2, NK, 0, 0>;
template <class C, int KT> DEVI void c_steps(f32x4 (&acc)[C::MF][C::NF], Frags<C>& cur, Frags<C>& nxt, Loader<C>& L, const RdAddr<C>& R, int wave, unsigned lds0,
                                             unsigned& bcur) {
  if constexpr (KT < C::NK) {
    constexpr int S = C::S, last = KT + S - 1 < C::NK - 1 ? KT + S - 1 : C::NK - 1;
    constexpr int n = KT + 1 < S ? 2 * (S - 2 - KT) + 3 * KT : 3 * (last - (KT + 1));
    XHook<(n > 0 ? n : 0)> hk;
    iter<C, 0xfu, (KT + S < C::NK), (KT + 1 < C::NK), 0, 10>(acc, cur, nxt, L, R, wave, lds0, bcur, hk, KT);
    c_steps<C, KT + 1>(acc, nxt, cur, L, R, wave, lds0, bcur);
  }
}
using CD = Cfg<256, 224, 32, 4, 4, 2, 1, 32, 0, 1>;
DEVI float geglu(float x, float u) {
  constexpr float K1 = -2.f * 0.7978845608028654f * 1.4426950408889634f, K2 = K1 * 0.044715f;
  float e = __builtin_amdgcn_exp2f(x * (K1 + K2 * x * x));
  return x * __builtin_amdgcn_rcpf(1.f + e) * u;
}
struct HookD : PfHook<1> {
  char* xch; unsigned xo; int k2;
  DEVI void tail() const {
    glds4(v, p, l);
  }
  DEVI void mid(int kt) const {
  }
};
using CE = Cfg<128, 128, 64, 4, 2, 2, 2, FF / 128, 0, 0>;
DEVI void publish(char* ws, int tid, unsigned mine) {
  asm volatile("s_waitcnt vmcnt(0)" ::: "memory");
  bar();
  if (tid < 32) __hip_atomic_store((gu32*)(ws + WS_FLG + mine * 128 + tid * 4), 1u, __ATOMIC_RELAXED, __HIP_MEMORY_SCOPE_AGENT);
  asm volatile("" ::: "memory");
}
template <int SL> DEVI void await(char* ws, int wave, unsigned watch) {
  if (wave == 0) {
    if (SL) __builtin_amdgcn_s_sleep(SL);
    gu32* pf = (gu32*)(ws + WS_FLG + watch * 128);
    for (int spin = 0; spin < (1 << 20); ++spin) {
      if (__builtin_amdgcn_ballot_w64(__hip_atomic_load(pf, __ATOMIC_RELAXED, __HIP_MEMORY_SCOPE_AGENT) != 1u) == 0) break;
      __builtin_amdgcn_s_sleep(1);
    }
  }
  asm volatile("" ::: "memory");
  bar();
}
__global__ __launch_bounds__(512, 2) void kF(const void* __restrict__ w, const void* __restrict__ xres, const void* __restrict__ wg, const void* __restrict__ wu,
                                             const void* __restrict__ wd, void* __restrict__ out, char* __restrict__ ws) {
  using C = CR<DM / 64>;
  static_assert(C::NP == 3 && C::PA == 16 && C::P == 24, "piece layout");
  STAMP(1, 0);
  __shared__ __attribute__((aligned(256))) char sm[C::LDS + 4096];
  unsigned lds0 = (unsigned)(size_t)sm, ldb = lds0 + C::LDS;
  int tid = threadIdx.x, lane = tid & 63, wave = __builtin_amdgcn_readfirstlane(tid >> 6);
  int b = blockIdx.x, xcd = b & 7, idx = b >> 3;
  int g = lane >> 4, r15 = lane & 15;
  int dtm = (xcd >> 2) * 4 + (idx & 3), dtn = (xcd & 3) * 8 + (idx >> 2);
  Loader<CD> LD;
  RdAddr<CD> RD;
  float cv[8];
  {
  int wk = wave & 1, wm = wave >> 1;
  int tm = (xcd >> 1) * 4 + (idx & 3), tn = (xcd & 1) * 8 + (idx >> 2);
  int mf = tm * 8 + wm * 2 + wk, row = mf * 16 + r15;
  int h = (b & 7) * 4 + ((b >> 3) & 3), rg = b >> 5;
  Loader<C> L;
  f32x4 res[2];
  {
    const f32x4* pp = wave < 4 ? (const f32x4*)(ws + WS_KVP) + h * 4096 + tid : (const f32x4*)(ws + WS_ZP) + h * 128 + lane;
    f32x4 p[16];
    if (wave < 4) {
#pragma unroll
      for (int i = 0; i < 16; ++i) p[i] = gload4(pp + i * 256);
    } else {
      p[0] = gload4(pp); p[1] = gload4(pp + 64);
#pragma unroll
      for (int i = 2; i < 16; ++i) p[i] = f32x4{0, 0, 0, 0};
    }
    f32x4 q[2];
#pragma unroll
    for (int i = 0; i < 2; ++i) q[i] = gload4((const u32x4*)(ws + WS_QF) + ((rg * 16 + wave * 2 + i) * 32 + h) * 64 + lane);
    loader_init<C>(L, lds0, wave, lane, ws + WS_OT, tm * 128, DM, w, w, DM, 0, [&](int lc) { return tn * 64 + lc * 8; }, 0, 4u);
    prologue<C>(L, wave, lds0, 1, 4u);
#pragma unroll
    for (int jp = 0; jp < 2; ++jp) res[jp] = gload4((const char*)xres + ((size_t)row * DM + (tn * 8 + 2 * (jp * 2 + (g & 1)) + (g >> 1)) * 8) * 2);
    asm volatile("s_waitcnt vmcnt(%10)" : "+v"(p[0]), "+v"(p[1]), "+v"(p[2]), "+v"(p[3]), "+v"(p[4]), "+v"(p[5]), "+v"(p[6]), "+v"(p[7]), "+v"(p[8]), "+v"(p[9]) : "n"(C::S + 4) : "memory");
    asm volatile("" : "+v"(p[10]), "+v"(p[11]), "+v"(p[12]), "+v"(p[13]), "+v"(p[14]), "+v"(p[15])::"memory");
    f32x4 s = ((p[0] + p[1]) + (p[2] + p[3])) + ((p[4] + p[5]) + (p[6] + p[7])) + (((p[8] + p[9]) + (p[10] + p[11])) + ((p[12] + p[13]) + (p[14] + p[15])));
    if (wave < 4) {
      int fd = wave >> 1, e = (wave & 1) * 16 + r15;
      int fe = (e >> 2) & 1, i = ((e >> 3) << 2) | (e & 3);
      *LDS3(u32x2, ldb + ((fe * 64 + g * 16 + i) * 2 + fd) * 8) = u32x2{pack_bf16(s[0], s[1]), pack_bf16(s[2], s[3])};
    } else {
#pragma unroll
      for (int c = 0; c < 4; ++c) { s[c] = sum_g(s[c]); s[c] += __shfl_xor(s[c], 8); }
      if (wave == 4 && lane < 8) *LDS3(u32x2, ldb + 2048 + ((lane & 3) * 2 + (lane >> 2)) * 8) = u32x2{pack_bf16(s[0], s[1]), pack_bf16(s[2], s[3])};
    }
    wait_lgkm0(); bar();
    asm volatile("s_waitcnt vmcnt(%2)" : "+v"(q[0]), "+v"(q[1]) : "n"(C::S + 2) : "memory");
    bf16x8 k0 = lds_b128(ldb + lane * 16), k1 = lds_b128(ldb + 1024 + lane * 16), zf = lds_b128(ldb + 2048 + g * 16);
    f32x4 zero = {0, 0, 0, 0};
#pragma unroll
    for (int i = 0; i < 2; ++i) {
      bf16x8 qq = __builtin_bit_cast(bf16x8, q[i]);
      f32x4 n0 = mfma16(k0, qq, zero), n1 = mfma16(k1, qq, zero), dn = mfma16(zf, qq, zero);
      float inv = 1.f / (dn[0] + 1e-6f);
      u32x4 o = {pack_bf16(n0[0] * inv, n0[1] * inv), pack_bf16(n0[2] * inv, n0[3] * inv), pack_bf16(n1[0] * inv, n1[1] * inv), pack_bf16(n1[2] * inv, n1[3] * inv)};
      st16<true>(ws + WS_OT, 4 * MB, (((rg * 16 + wave * 2 + i) * 128 + h * 4) * 16 + lane) * 16, o);
    }
  }
  RdAddr<C> R; rd_init<C>(R, lds0, wm, 0, wk, lane, wk, 0, 0);
  STAMP(1, 1);
  publish(ws, tid, b);
  loader_init<CD>(LD, lds0, wave, lane, ws + WS_HT, dtm * 256, DM, wg, wu, FF, 0, [&](int lc) { return dtn * 112 + lc * 8; }, 0, 0u);
  asm volatile("" : "+v"(LD.voff[0]), "+v"(LD.voff[1]), "+v"(LD.voff[2]), "+v"(LD.voff[3]));
  await<0>(ws, wave, (tm >> 1) * 32 + (lane & 31));
  STAMP(1, 2); STAMP(2, 0);
  prologue<C>(L, wave, lds0, 0, 3u);
  f32x4 acc[2][4];
#pragma unroll
  for (int m = 0; m < 2; ++m)
#pragma unroll
    for (int j = 0; j < 4; ++j) acc[m][j] = f32x4{0, 0, 0, 0};
  {
    Frags<C> f0, f1;
    wait_vm<2 * (C::S - 1)>();
    bar();
    read_all<C>(f0, R, 0);
    unsigned bcur = 0;
    c_steps<C, 0>(acc, f0, f1, L, R, wave, lds0, bcur);
  }
  asm volatile("" : "+v"(res[0]), "+v"(res[1]));
  STAMP(2, 1);
  unsigned scr = lds0 + (wave >> 2) * CD::STAGE + (wave & 3) * 4096 + lane * 16;
#pragma unroll
  for (int j = 0; j < 4; ++j) *LDS3(f32x4, scr + j * 1024) = acc[1][j];
  wait_lgkm0(); bar();
#pragma unroll
  for (int j = 0; j < 4; ++j) acc[0][j] += *LDS3(f32x4, (scr + (wk ? -4096 : 4096)) + j * 1024);
  float ss = 0, sq = 0;
#pragma unroll
  for (int jp = 0; jp < 2; ++jp) {
    f32x4 a = acc[0][2 * jp], c = acc[0][2 * jp + 1];
    u32x4 rr = __builtin_bit_cast(u32x4, res[jp]);
#pragma unroll
    for (int r = 0; r < 4; ++r) { float u = a[r], v = c[r]; swap16(u, v); a[r] = u; c[r] = v; }
    u32x4 o;
    o[0] = pack_bf16(a[0] + bf_lo(rr[0]), a[1] + bf_hi(rr[0]));
    o[1] = pack_bf16(a[2] + bf_lo(rr[1]), a[3] + bf_hi(rr[1]));
    o[2] = pack_bf16(c[0] + bf_lo(rr[2]), c[1] + bf_hi(rr[2]));
    o[3] = pack_bf16(c[2] + bf_lo(rr[3]), c[3] + bf_hi(rr[3]));
    int c8 = tn * 8 + 2 * (jp * 2 + (g & 1)) + (g >> 1);
    st16<true>(ws + WS_HT, 4 * MB, ((mf * 128 + c8) * 16 + r15) * 16, o);
#pragma unroll
    for (int i = 0; i < 4; ++i) { ss = dot2(o[i], ONES2, ss); sq = dot2(o[i], o[i], sq); }
  }
  ss = sum_g(ss); sq = sum_g(sq);
  if (g == 0) __builtin_amdgcn_raw_buffer_store_b64(u32x2{__float_as_uint(ss), __float_as_uint(sq)}, __builtin_amdgcn_make_buffer_rsrc(ws + WS_ST, 0, SEQ * 128, 0x00020000), ((mf * 16 + tn) * 16 + r15) * 8, 0, 16);
  STAMPW(2, 2);
  publish(ws, tid, 256 + tm * 16 + tn);
  prologue<CD>(LD, wave, lds0, 0, 12u);
  {
    int ct = tid < 448 ? tid >> 1 : 223;
    const float* cp = (const float*)(ws + WS_CS) + (tid & 1) * 16 * FF + (ct < 112 ? dtn * 112 + ct : FF + dtn * 112 + ct - 112);
#pragma unroll
    for (int i = 0; i < 8; ++i) cv[i] = gload1(cp + i * 2 * FF);
  }
  rd_init<CD>(RD, lds0, wave >> 1, wave & 1, 0, lane, 0, 0, 0);
  asm volatile("" : "+v"(RD.a[0][0]), "+v"(RD.b[0]), "+v"(RD.b[1]), "+v"(RD.b[2]), "+v"(RD.b[3]), "+v"(RD.b[4]), "+v"(RD.b[5]), "+v"(RD.b[6]));
  await<0>(ws, wave, 256 + dtm * 32 + (lane & 31));
  }

  int kh = xcd & 1, etm = (xcd >> 1) * 4 + (idx & 3), etn = idx >> 2;
  Loader<CE> LE;
  {
    using C = CD;
    STAMP(3, 0);
    asm volatile("" : "+v"(tid)); lane = tid & 63; g = lane >> 4; r15 = lane & 15;
    constexpr unsigned O_ROW = CR<16>::LDS, O_CS = O_ROW + 2048;
    int wn = wave & 1, wm = wave >> 1, tm = dtm, tn = dtn;
    Loader<C>& L = LD;
    issue_piece<C>(L, 0, wave, lds0); issue_piece<C>(L, 1, wave, lds0);
    const char* sp = ws + WS_ST + (size_t)(tm * 256 + wave * 32) * 128 + lane * 16;
    f32x4 sv[4];
#pragma unroll
    for (int i = 0; i < 4; ++i) sv[i] = gload4c(sp + i * 1024);
    int ct = tid < 448 ? tid >> 1 : 223;
    prologue<C>(L, wave, lds0, 1, 3u);
    wait_vm<2 * (C::S - 1)>();
    asm volatile("" : "+v"(sv[0]), "+v"(sv[1]), "+v"(sv[2]), "+v"(sv[3]), "+v"(cv[0]), "+v"(cv[1]), "+v"(cv[2]), "+v"(cv[3]), "+v"(cv[4]), "+v"(cv[5]), "+v"(cv[6]), "+v"(cv[7]));
    {
      f32x4 A = sv[0] + sv[1], B = sv[2] + sv[3], t;
      bool o1 = lane & 32, o2 = lane & 16;
#pragma unroll
      for (int i = 0; i < 4; ++i) t[i] = (o1 ? B[i] : A[i]) + __shfl_xor(o1 ? A[i] : B[i], 32);
      float ss = (o2 ? t[2] : t[0]) + __shfl_xor(o2 ? t[0] : t[2], 16), qq = (o2 ? t[3] : t[1]) + __shfl_xor(o2 ? t[1] : t[3], 16);
      ss += __shfl_xor(ss, 8); qq += __shfl_xor(qq, 8);
      float mean = ss * (1.f / DM), var = fmaxf(qq * (1.f / DM) - mean * mean, 0.f);
      float rstd = __builtin_amdgcn_rsqf(var + 1e-5f);
      if (!(lane & 8)) *LDS3(f32x2, lds0 + O_ROW + (wave * 32 + (lane >> 5) * 16 + (lane & 7) * 2 + ((lane >> 4) & 1)) * 8) = f32x2{mean, rstd};
    }
    float cs = ((cv[0] + cv[1]) + (cv[2] + cv[3])) + ((cv[4] + cv[5]) + (cv[6] + cv[7]));
    cs += __shfl_xor(cs, 1);
    if (tid < 448 && !(tid & 1)) *LDS3(float, lds0 + O_CS + ct * 4) = cs;
    RdAddr<C>& R = RD;
    f32x4 acc[4][7];
#pragma unroll
    for (int m = 0; m < 4; ++m)
#pragma unroll
      for (int j = 0; j < 7; ++j) acc[m][j] = f32x4{0, 0, 0, 0};
    HookD hk; hk.xch = ws + WS_XCH; hk.xo = ((b * 8 + wave) * 256 + lane) * 16; hk.k2 = 4 + (wave & (FST - 1));
    hk.v = lane * 64; hk.l = lds0 + O_ROW + 3072; hk.p = uni_ptr((const char*)wd + (size_t)b * 28672 + (wave < 7 ? wave : 6) * 4096);
    hk.k1 = wave < 7 ? 4 + 3 * wave : -1;
#pragma unroll
    for (int i = 0; i < 4; ++i) st16<FWT>(ws + WS_XCH, 8 * MB, (((b * 8 + wave) * 4 + i) * 64 + lane) * 16, u32x4{~0u, ~0u, ~0u, ~0u});
    mainloop<C, 0x7fu, HookD, true>(acc, L, R, wave, lds0, hk);
    asm volatile("" : "+v"(tid)); lane = tid & 63; g = lane >> 4; r15 = lane & 15;
    STAMP(3, 1);
    int hi = g >> 1;
    f32x2 st[2];
#pragma unroll
    for (int p = 0; p < 2; ++p) st[p] = *LDS3(f32x2, lds0 + O_ROW + (wm * 64 + (p * 2 + hi) * 16 + r15) * 8);
    int smf = tm * 16 + wm * 4 + ((g & 1) * 2 + hi);
    u32x4* dst = (u32x4*)(ws + WS_ACT) + ((size_t)smf * (FF / 8) + tn * 14 + wn * 7) * 16 + r15;
    const __amdgpu_buffer_rsrc_t drs = __builtin_amdgcn_make_buffer_rsrc(ws + WS_ACT, 0, SEQ * FF * 2, 0x00020000);
    unsigned doff = ((smf * (FF / 8) + tn * 14 + wn * 7) * 16 + r15) * 16;
#pragma unroll
    for (int j = 0; j < 7; ++j) {
      unsigned ca = lds0 + O_CS + ((wn * 7 + j) * 8 + (g & 1) * 4) * 4;
      f32x4 cg = *LDS3(f32x4, ca), cu = *LDS3(f32x4, ca + 448);
      unsigned pk[2][2];
#pragma unroll
      for (int p = 0; p < 2; ++p) {
        float a[4];
#pragma unroll
        for (int r = 0; r < 4; ++r) {
          float x = acc[2 * p][j][r], y = acc[2 * p + 1][j][r];
          swap32(x, y);
          a[r] = geglu((x - st[p][0] * cg[r]) * st[p][1], (y - st[p][0] * cu[r]) * st[p][1]);
        }
        pk[p][0] = pack_bf16(a[0], a[1]); pk[p][1] = pack_bf16(a[2], a[3]);
      }
      swap16u(pk[0][0], pk[1][0]); swap16u(pk[0][1], pk[1][1]);
      __builtin_amdgcn_raw_buffer_store_b128(u32x4{pk[0][0], pk[0][1], pk[1][0], pk[1][1]}, drs, doff + j * 256, 0, DST);
    }
  }
  loader_init<CE>(LE, lds0, wave, lane, ws + WS_ACT, etm * 128, FF, wd, wd, DM, kh * (FF / 2), [&](int lc) { return etn * 128 + lc * 8; }, 2 * kh, 0u);
  STAMPW(3, 2);
  publish(ws, tid, 512 + dtm * 32 + dtn);
  if (!EPF) prologue<CE>(LE, wave, lds0, 0, 12u);
  RdAddr<CE> RE; rd_init<CE>(RE, lds0, wave >> 2, (wave >> 1) & 1, wave & 1, lane, 0, 0, 2 * (wave & 1));
  asm volatile("" : "+v"(RE.a[0][0]), "+v"(RE.b[0]), "+v"(RE.b[1]), "+v"(RE.b[2]), "+v"(RE.b[3]));
  await<0>(ws, wave, 512 + (lane < 16 ? (etm >> 1) * 32 + kh * 16 + lane : dtm * 32 + (dtn ^ 8)));

  {
  using C = CE;
  STAMP(4, 0);
  asm volatile("" : "+v"(tid)); lane = tid & 63; g = lane >> 4; r15 = lane & 15;
  Loader<C>& L = LE;
  prologue<C>(L, wave, lds0, 0, 3u);
  int wk = wave & 1, wn = (wave >> 1) & 1, wm = wave >> 2, tm = etm, tn = etn;
  int c8 = tn * 16 + wn * 8 + 2 * (2 * wk + (g & 1)) + (g >> 1);
  int mf0 = tm * 8 + wm * 4 + 2 * kh;
  RdAddr<C>& R = RE;
  f32x4 acc[4][4];
#pragma unroll
  for (int m = 0; m < 4; ++m)
#pragma unroll
    for (int j = 0; j < 4; ++j) acc[m][j] = f32x4{0, 0, 0, 0};
  NoHook hk;
  mainloop<C, 0xfu, NoHook, true>(acc, L, R, wave, lds0, hk);
  STAMP(4, 1);
  u32x4 res[2];
#pragma unroll
  for (int m = 0; m < 2; ++m) res[m] = __builtin_amdgcn_raw_buffer_load_b128(__builtin_amdgcn_make_buffer_rsrc(ws + WS_HT, 0, 4 * MB, 0x00020000), (((mf0 + m) * 128 + c8) * 16 + r15) * 16, 0, 16);
#pragma unroll
  for (int m = 3; m >= 0; --m)
#pragma unroll
    for (int j = 0; j < 2; ++j) *LDS3(f32x4, lds0 + ((wave * 8 + m * 2 + j) * 64 + lane) * 16) = acc[m][2 + j];
  wait_lgkm0(); bar();
  const __amdgpu_buffer_rsrc_t rs = __builtin_amdgcn_make_buffer_rsrc(ws + WS_XCH, 0, 256 * 8 * 4096, 0x00020000);
#pragma unroll
  for (int m = 3; m >= 0; --m)
#pragma unroll
    for (int j = 0; j < 2; ++j) {
      acc[m][j] += *LDS3(f32x4, lds0 + (((wave ^ 1) * 8 + m * 2 + j) * 64 + lane) * 16);
      if (m >= 2) {
        u32x4 u = __builtin_bit_cast(u32x4, acc[m][j]);
#pragma unroll
        for (int r = 0; r < 4; ++r) u[r] = min(u[r], 0xfffffffeu);
        __builtin_amdgcn_raw_buffer_store_b128(u, rs, (((b * 8 + wave) * 4 + (m - 2) * 2 + j) * 64 + lane) * 16, 0, 16);
      }
    }
  u32x4 x[4];
  if (ESL0) __builtin_amdgcn_s_sleep(ESL0);
  for (int spin = 0; spin < (1 << 20); ++spin) {
    asm volatile("" ::: "memory");
#pragma unroll
    for (int i = 0; i < 4; ++i) x[i] = __builtin_amdgcn_raw_buffer_load_b128(rs, ((((b ^ 1) * 8 + wave) * 4 + i) * 64 + lane) * 16, 0, 16);
    unsigned mx = 0;
#pragma unroll
    for (int i = 0; i < 4; ++i) mx = max(max(mx, max(x[i][0], x[i][1])), max(x[i][2], x[i][3]));
    if (__builtin_amdgcn_ballot_w64(mx == 0xffffffffu) == 0) break;
    if (ESL) __builtin_amdgcn_s_sleep(ESL);
  }
#pragma unroll
  for (int m = 0; m < 2; ++m) {
    f32x4 a = acc[m][0] + __builtin_bit_cast(f32x4, x[2 * m]), c = acc[m][1] + __builtin_bit_cast(f32x4, x[2 * m + 1]);
#pragma unroll
    for (int r = 0; r < 4; ++r) { float u = a[r], v = c[r]; swap16(u, v); a[r] = u; c[r] = v; }
    u32x4 o;
    o[0] = pack_bf16(a[0] + bf_lo(res[m][0]), a[1] + bf_hi(res[m][0]));
    o[1] = pack_bf16(a[2] + bf_lo(res[m][1]), a[3] + bf_hi(res[m][1]));
    o[2] = pack_bf16(c[0] + bf_lo(res[m][2]), c[1] + bf_hi(res[m][2]));
    o[3] = pack_bf16(c[2] + bf_lo(res[m][3]), c[3] + bf_hi(res[m][3]));
    st16<(WTM & 8) != 0>(out, 4 * MB, (((mf0 + m) * 16 + r15) * DM + c8 * 8) * 2, o);
  }
  STAMPW(4, 2);
  }
}

extern "C" void kernel_launch(void* const* d_in, const int* in_sizes, int n_in, void* d_out, int out_size, void* d_ws, size_t ws_size,
                              hipStream_t stream) {
  char* ws = (char*)d_ws;
  kA<<<256, 512, 0, stream>>>(d_in[0], d_in[1], d_in[2], d_in[4], d_in[5], d_in[6], ws);
  kF<<<256, 512, 0, stream>>>(d_in[2], d_in[0], d_in[4], d_in[5], d_in[6], d_out, ws);
}
```
